```python
import torch

def _softmax(x, axis=-1):
    return torch.softmax(x, dim=axis)
def _gelu(x):
    return torch.nn.functional.gelu(x, approximate="tanh")
def _silu(x):
    return torch.nn.functional.silu(x)
def _sigmoid(x):
    return torch.sigmoid(x)
def _layernorm(x, axis=-1, eps=1e-5):
    m = x.mean(dim=axis, keepdim=True)
    v = x.var(dim=axis, keepdim=True, unbiased=False)
    return (x - m) / torch.sqrt(v + eps)
def _scatter_add(tgt, idx, src, axis=0):
    return tgt.clone().index_add_(axis, idx, src)
def _segment_sum(src, seg, n):
    out = torch.zeros((n,) + src.shape[1:], dtype=src.dtype)
    return out.index_add_(0, seg, src)
def _pad(x, pw):
    flat = tuple(v for pair in reversed(pw) for v in pair)
    return torch.nn.functional.pad(x, flat)

kernel_name = 'standalone__mha_softmax__near_ridge'

def setup_inputs():

    _w = (lambda a: (lambda t: t.to(torch.bfloat16) if t.is_floating_point() else t)(torch.as_tensor(a)))
    B, H, Sq, Sk, D = 32, 16, 384, 384, 256
    return {
        "q": _w((lambda *s: torch.randn(*s, dtype=torch.bfloat16) * 0.8)(B, H, Sq, D)),
        "k": _w((lambda *s: torch.randn(*s, dtype=torch.bfloat16) * 0.8)(B, H, Sk, D)),
        "v": _w((lambda *s: torch.randn(*s, dtype=torch.bfloat16) * 0.05)(B, H, Sk, D)),
    }

def reference(q, k, v):
    scale = q.shape[-1] ** -0.5
    scores = torch.einsum("bhqd,bhkd->bhqk", q, k) * scale
    p = _softmax(scores, axis=-1)
    return torch.einsum("bhqk,bhkd->bhqd", p, v)
```

```cpp
#include <hip/hip_runtime.h>
#include <hip/hip_bf16.h>

namespace {
constexpr unsigned kTensorBytes = 512u * 384u * 256u * 2u;
constexpr int kLdsBytes = 131072 + 4 * 4352;

__global__ __launch_bounds__(256, 1) void mha_softmax_fwd(const void* __restrict__ q, const void* __restrict__ k, const void* __restrict__ v, void* __restrict__ o) {
  extern __shared__ __attribute__((aligned(16))) char lds[];
  const unsigned tid = threadIdx.x, l = tid & 63u;
  const unsigned w = __builtin_amdgcn_readfirstlane(tid >> 6);
  const unsigned ldsb = (unsigned)(size_t)lds;
  const unsigned bid = blockIdx.x, x = bid & 7u, j = bid >> 3;
  const unsigned head = (j >> 1) * 8u + x;
  const unsigned hoff = head * (384u * 512u);
  const unsigned hk = hoff + w * 4096u, hv = hoff + w * 8192u, q0 = hoff + ((j & 1u) * 192u + w * 48u) * 512u, sw = ldsb + w * 4096u;
  const unsigned row = l & 15u, g = l >> 4;
  unsigned kb0 = ldsb + (row >> 3) * 4096u + (row & 7u) * 128u + 16u * (g ^ ((row >> 1) & 7u)), kb1 = kb0 ^ 64u;
  unsigned qb0 = kb0 + (row >> 3) * 12288u + w * 4096u, qb1 = kb1 + (row >> 3) * 12288u + w * 4096u;
  const unsigned key7 = 4u * (g & 1u) + ((l >> 2) & 3u);
  const unsigned vbl = ldsb + (g >> 1) * 2048u + key7 * 128u + 8u * (l & 3u);
  unsigned vb0 = vbl + 32u * (0u ^ (key7 >> 1)), vb1 = vbl + 32u * (1u ^ (key7 >> 1)), vb2 = vbl + 32u * (2u ^ (key7 >> 1)), vb3 = vbl + 32u * (3u ^ (key7 >> 1));
  const unsigned voe = (l >> 3) * 512u + 16u * ((l & 7u) ^ (l >> 4)), voo = voe ^ 64u, vok = voe ^ ((w & 1u) * 64u);
  const unsigned vov = (l >> 3) * 512u + 16u * ((l & 7u) ^ ((l >> 3) & 6u));
  const unsigned stw = ldsb + 131072u + w * 4352u + row * 272u + 8u * g, str = ldsb + 131072u + w * 4352u + g * 272u + 16u * row;
  const unsigned vos = g * 512u + row * 16u;
  const __amdgpu_buffer_rsrc_t srq = __builtin_amdgcn_make_buffer_rsrc((void*)q, 0, (int)kTensorBytes, 0x00020000);
  const __amdgpu_buffer_rsrc_t srk = __builtin_amdgcn_make_buffer_rsrc((void*)k, 0, (int)kTensorBytes, 0x00020000);
  const __amdgpu_buffer_rsrc_t srv = __builtin_amdgcn_make_buffer_rsrc((void*)v, 0, (int)kTensorBytes, 0x00020000);
  const __amdgpu_buffer_rsrc_t sro = __builtin_amdgcn_make_buffer_rsrc(o, 0, (int)kTensorBytes, 0x00020000);
  asm volatile(
    "s_nop 4\n"
    "s_mov_b32 s48, %[sk]\n"
    "s_cmp_eq_u32 s48, 0\n"
    "s_cbranch_scc1 5f\n"
    "4:\n"
    "s_sleep 8\n"
    "s_sub_u32 s48, s48, 1\n"
    "s_cmp_lg_u32 s48, 0\n"
    "s_cbranch_scc1 4b\n"
    "5:\n"
    "s_mov_b32 s36, %[hk]\n"
    "s_add_u32 s37, %[hk], 0x1800000\n"
    "s_mov_b32 s38, %[hv]\n"
    "s_mov_b32 s40, %[q0]\n"
    "s_add_u32 s39, %[q0], 0x1800000\n"
    "s_mov_b32 s41, 0xf0000000\n"
    "s_mov_b32 s42, %[sw]\n"
    "s_mov_b32 s43, 0x3db8aa3b\n"
    "s_mov_b32 s44, 0xbdb8aa3b\n"
    "s_mov_b32 s45, 0\n"
    "v_mov_b32 v0, 0x3f803f80\n"
    "v_accvgpr_write_b32 a224, v0\n"
    "v_accvgpr_write_b32 a225, v0\n"
    "v_accvgpr_write_b32 a226, v0\n"
    "v_accvgpr_write_b32 a227, v0\n"
    "s_add_u32 s46, s40, 0x0\n"
    "s_add_u32 m0, s42, 0x8000\n"
    "s_nop 0\n"
    "buffer_load_dwordx4 %[voe], %[srq], s46 offen nt lds\n"
    "s_add_u32 m0, s42, 0x8380\n"
    "s_nop 0\n"
    "buffer_load_dwordx4 %[voe], %[srq], s46 offen offset:128 nt lds\n"
    "s_add_u32 m0, s42, 0x8700\n"
    "s_nop 0\n"
    "buffer_load_dwordx4 %[voe], %[srq], s46 offen offset:256 nt lds\n"
    "s_add_u32 m0, s42, 0x8a80\n"
    "s_nop 0\n"
    "buffer_load_dwordx4 %[voe], %[srq], s46 offen offset:384 nt lds\n"
    "s_add_u32 s46, s40, 0x1000\n"
    "s_add_u32 m0, s42, 0xc000\n"
    "s_nop 0\n"
    "buffer_load_dwordx4 %[voo], %[srq], s46 offen nt lds\n"
    "s_add_u32 m0, s42, 0xc380\n"
    "s_nop 0\n"
    "buffer_load_dwordx4 %[voo], %[srq], s46 offen offset:128 nt lds\n"
    "s_add_u32 m0, s42, 0xc700\n"
    "s_nop 0\n"
    "buffer_load_dwordx4 %[voo], %[srq], s46 offen offset:256 nt lds\n"
    "s_add_u32 m0, s42, 0xca80\n"
    "s_nop 0\n"
    "buffer_load_dwordx4 %[voo], %[srq], s46 offen offset:384 nt lds\n"
    "s_add_u32 s46, s36, 0x0\n"
    "s_add_u32 m0, s42, 0x4000\n"
    "s_nop 0\n"
    "buffer_load_dwordx4 %[vok], %[srk], s46 offen nt lds\n"
    "s_add_u32 m0, s42, 0x4380\n"
    "s_nop 0\n"
    "buffer_load_dwordx4 %[vok], %[srk], s46 offen offset:128 nt lds\n"
    "s_add_u32 m0, s42, 0x4700\n"
    "s_nop 0\n"
    "buffer_load_dwordx4 %[vok], %[srk], s46 offen offset:256 nt lds\n"
    "s_add_u32 m0, s42, 0x4a80\n"
    "s_nop 0\n"
    "buffer_load_dwordx4 %[vok], %[srk], s46 offen offset:384 nt lds\n"
    "s_add_u32 s46, s40, 0x2000\n"
    "s_add_u32 m0, s42, 0x10000\n"
    "s_nop 0\n"
    "buffer_load_dwordx4 %[voe], %[srq], s46 offen nt lds\n"
    "s_add_u32 m0, s42, 0x10380\n"
    "s_nop 0\n"
    "buffer_load_dwordx4 %[voe], %[srq], s46 offen offset:128 nt lds\n"
    "s_add_u32 m0, s42, 0x10700\n"
    "s_nop 0\n"
    "buffer_load_dwordx4 %[voe], %[srq], s46 offen offset:256 nt lds\n"
    "s_add_u32 m0, s42, 0x10a80\n"
    "s_nop 0\n"
    "buffer_load_dwordx4 %[voe], %[srq], s46 offen offset:384 nt lds\n"
    "s_add_u32 s46, s40, 0x3000\n"
    "s_add_u32 m0, s42, 0x14000\n"
    "s_nop 0\n"
    "buffer_load_dwordx4 %[voo], %[srq], s46 offen nt lds\n"
    "s_add_u32 m0, s42, 0x14380\n"
    "s_nop 0\n"
    "buffer_load_dwordx4 %[voo], %[srq], s46 offen offset:128 nt lds\n"
    "s_add_u32 m0, s42, 0x14700\n"
    "s_nop 0\n"
    "buffer_load_dwordx4 %[voo], %[srq], s46 offen offset:256 nt lds\n"
    "s_add_u32 m0, s42, 0x14a80\n"
    "s_nop 0\n"
    "buffer_load_dwordx4 %[voo], %[srq], s46 offen offset:384 nt lds\n"
    "s_add_u32 s46, s40, 0x4000\n"
    "s_add_u32 m0, s42, 0x18000\n"
    "s_nop 0\n"
    "buffer_load_dwordx4 %[voe], %[srq], s46 offen nt lds\n"
    "s_add_u32 m0, s42, 0x18380\n"
    "s_nop 0\n"
    "buffer_load_dwordx4 %[voe], %[srq], s46 offen offset:128 nt lds\n"
    "s_add_u32 m0, s42, 0x18700\n"
    "s_nop 0\n"
    "buffer_load_dwordx4 %[voe], %[srq], s46 offen offset:256 nt lds\n"
    "s_add_u32 m0, s42, 0x18a80\n"
    "s_nop 0\n"
    "buffer_load_dwordx4 %[voe], %[srq], s46 offen offset:384 nt lds\n"
    "s_add_u32 s46, s40, 0x5000\n"
    "s_add_u32 m0, s42, 0x1c000\n"
    "s_nop 0\n"
    "buffer_load_dwordx4 %[voo], %[srq], s46 offen nt lds\n"
    "s_add_u32 m0, s42, 0x1c380\n"
    "s_nop 0\n"
    "buffer_load_dwordx4 %[voo], %[srq], s46 offen offset:128 nt lds\n"
    "s_add_u32 m0, s42, 0x1c700\n"
    "s_nop 0\n"
    "buffer_load_dwordx4 %[voo], %[srq], s46 offen offset:256 nt lds\n"
    "s_add_u32 m0, s42, 0x1ca80\n"
    "s_nop 0\n"
    "buffer_load_dwordx4 %[voo], %[srq], s46 offen offset:384 nt lds\n"
    "s_getpc_b64 s[50:51]\n"
    "8:\n"
    "v_mbcnt_lo_u32_b32 v1, -1, 0\n"
    "v_mbcnt_hi_u32_b32 v1, -1, v1\n"
    "v_lshlrev_b32 v1, 7, v1\n"
    "s_lshl_b32 s48, %[wv], 13\n"
    "v_add_u32 v1, s48, v1\n"
    "v_min_u32 v1, 9f-8b-4, v1\n"
    "global_load_dword v239, v1, s[50:51]\n"
    "s_waitcnt vmcnt(21)\n"
    "ds_read_b128 a[96:99], %[qb0] offset:32768\n"
    "ds_read_b128 a[100:103], %[qb1] offset:32768\n"
    "ds_read_b128 a[104:107], %[qb0] offset:33792\n"
    "ds_read_b128 a[108:111], %[qb1] offset:33792\n"
    "ds_read_b128 a[112:115], %[qb0] offset:34816\n"
    "ds_read_b128 a[116:119], %[qb1] offset:34816\n"
    "ds_read_b128 a[120:123], %[qb0] offset:35840\n"
    "ds_read_b128 a[124:127], %[qb1] offset:35840\n"
    "s_waitcnt lgkmcnt(0)\n"
    "s_add_u32 s46, s36, 0x8000\n"
    "s_add_u32 m0, s42, 0xc000\n"
    "s_nop 0\n"
    "buffer_load_dwordx4 %[vok], %[srk], s46 offen nt lds\n"
    "s_add_u32 m0, s42, 0xc380\n"
    "s_nop 0\n"
    "buffer_load_dwordx4 %[vok], %[srk], s46 offen offset:128 nt lds\n"
    "s_add_u32 m0, s42, 0xc700\n"
    "s_nop 0\n"
    "buffer_load_dwordx4 %[vok], %[srk], s46 offen offset:256 nt lds\n"
    "s_add_u32 m0, s42, 0xca80\n"
    "s_nop 0\n"
    "buffer_load_dwordx4 %[vok], %[srk], s46 offen offset:384 nt lds\n"
    "s_add_u32 s46, s36, 0x4000\n"
    "s_add_u32 m0, s42, 0x8000\n"
    "s_nop 0\n"
    "buffer_load_dwordx4 %[vok], %[srk], s46 offen nt lds\n"
    "s_add_u32 m0, s42, 0x8380\n"
    "s_nop 0\n"
    "buffer_load_dwordx4 %[vok], %[srk], s46 offen offset:128 nt lds\n"
    "s_add_u32 m0, s42, 0x8700\n"
    "s_nop 0\n"
    "buffer_load_dwordx4 %[vok], %[srk], s46 offen offset:256 nt lds\n"
    "s_add_u32 m0, s42, 0x8a80\n"
    "s_nop 0\n"
    "buffer_load_dwordx4 %[vok], %[srk], s46 offen offset:384 nt lds\n"
    "s_waitcnt vmcnt(17)\n"
    "v_add_u32 %[qb0], 0x10000, %[qb0]\n"
    "v_add_u32 %[qb1], 0x10000, %[qb1]\n"
    "ds_read_b128 a[128:131], %[qb0] offset:0\n"
    "ds_read_b128 a[132:135], %[qb1] offset:0\n"
    "ds_read_b128 a[136:139], %[qb0] offset:1024\n"
    "ds_read_b128 a[140:143], %[qb1] offset:1024\n"
    "ds_read_b128 a[144:147], %[qb0] offset:2048\n"
    "ds_read_b128 a[148:151], %[qb1] offset:2048\n"
    "ds_read_b128 a[152:155], %[qb0] offset:3072\n"
    "ds_read_b128 a[156:159], %[qb1] offset:3072\n"
    "s_waitcnt lgkmcnt(0)\n"
    "s_add_u32 s46, s36, 0x14000\n"
    "s_add_u32 m0, s42, 0x14000\n"
    "s_nop 0\n"
    "buffer_load_dwordx4 %[vok], %[srk], s46 offen nt lds\n"
    "s_add_u32 m0, s42, 0x14380\n"
    "s_nop 0\n"
    "buffer_load_dwordx4 %[vok], %[srk], s46 offen offset:128 nt lds\n"
    "s_add_u32 m0, s42, 0x14700\n"
    "s_nop 0\n"
    "buffer_load_dwordx4 %[vok], %[srk], s46 offen offset:256 nt lds\n"
    "s_add_u32 m0, s42, 0x14a80\n"
    "s_nop 0\n"
    "buffer_load_dwordx4 %[vok], %[srk], s46 offen offset:384 nt lds\n"
    "s_add_u32 s46, s36, 0x10000\n"
    "s_add_u32 m0, s42, 0x10000\n"
    "s_nop 0\n"
    "buffer_load_dwordx4 %[vok], %[srk], s46 offen nt lds\n"
    "s_add_u32 m0, s42, 0x10380\n"
    "s_nop 0\n"
    "buffer_load_dwordx4 %[vok], %[srk], s46 offen offset:128 nt lds\n"
    "s_add_u32 m0, s42, 0x10700\n"
    "s_nop 0\n"
    "buffer_load_dwordx4 %[vok], %[srk], s46 offen offset:256 nt lds\n"
    "s_add_u32 m0, s42, 0x10a80\n"
    "s_nop 0\n"
    "buffer_load_dwordx4 %[vok], %[srk], s46 offen offset:384 nt lds\n"
    "s_waitcnt vmcnt(17)\n"
    "ds_read_b128 a[64:67], %[qb0] offset:32768\n"
    "ds_read_b128 a[68:71], %[qb1] offset:32768\n"
    "ds_read_b128 a[72:75], %[qb0] offset:33792\n"
    "ds_read_b128 a[76:79], %[qb1] offset:33792\n"
    "ds_read_b128 a[80:83], %[qb0] offset:34816\n"
    "ds_read_b128 a[84:87], %[qb1] offset:34816\n"
    "ds_read_b128 a[88:91], %[qb0] offset:35840\n"
    "ds_read_b128 a[92:95], %[qb1] offset:35840\n"
    "s_waitcnt lgkmcnt(0)\n"
    "s_add_u32 s46, s36, 0xc000\n"
    "s_add_u32 m0, s42, 0x18000\n"
    "s_nop 0\n"
    "buffer_load_dwordx4 %[vok], %[srk], s46 offen nt lds\n"
    "s_add_u32 m0, s42, 0x18380\n"
    "s_nop 0\n"
    "buffer_load_dwordx4 %[vok], %[srk], s46 offen offset:128 nt lds\n"
    "s_add_u32 m0, s42, 0x18700\n"
    "s_nop 0\n"
    "buffer_load_dwordx4 %[vok], %[srk], s46 offen offset:256 nt lds\n"
    "s_add_u32 m0, s42, 0x18a80\n"
    "s_nop 0\n"
    "buffer_load_dwordx4 %[vok], %[srk], s46 offen offset:384 nt lds\n"
    "v_add_u32 %[qb0], 0xffff0000, %[qb0]\n"
    "v_add_u32 %[qb1], 0xffff0000, %[qb1]\n"
    "v_add_u32 %[vb0], 0x10000, %[vb0]\n"
    "v_add_u32 %[vb1], 0x10000, %[vb1]\n"
    "v_add_u32 %[vb2], 0x10000, %[vb2]\n"
    "v_add_u32 %[vb3], 0x10000, %[vb3]\n"
    "s_waitcnt vmcnt(37)\n"
    "s_barrier\n"
    "ds_read_b128 a[192:195], %[kb0] offset:16384\n"
    "ds_read_b128 a[196:199], %[kb1] offset:16384\n"
    "ds_read_b128 a[200:203], %[kb0] offset:17408\n"
    "ds_read_b128 a[204:207], %[kb1] offset:17408\n"
    "ds_read_b128 a[208:211], %[kb0] offset:18432\n"
    "s_waitcnt lgkmcnt(0)\n"
    ".p2align 6\n"
    "1:\n"
    "s_lshr_b32 s48, 6, s45\n"
    "s_bitcmp1_b32 s48, 0\n"
    "s_cselect_b64 vcc, -1, 0\n"
    "s_add_u32 s46, s36, 0x28000\n"
    "ds_read_b128 a[212:215], %[kb1] offset:18432\n"
    "ds_read_b128 a[216:219], %[kb0] offset:19456\n"
    "v_mfma_f32_16x16x32_bf16 v[0:3], a[192:195], a[96:99], 0\n"
    "s_add_u32 m0, s42, 0x0\n"
    "v_accvgpr_mov_b32 a0, a96\n"
    "v_mul_f32 v76, v219, v76\n"
    "v_mfma_f32_16x16x32_bf16 v[0:3], a[196:199], a[100:103], v[0:3]\n"
    "buffer_load_dwordx4 %[vok], %[srk], s46 offen nt lds\n"
    "v_mfma_f32_16x16x32_bf16 v[8:11], a[192:195], a[128:131], 0\n"
    "s_add_u32 m0, s42, 0x380\n"
    "v_accvgpr_mov_b32 a1, a97\n"
    "v_mul_f32 v77, v219, v77\n"
    "v_mfma_f32_16x16x32_bf16 v[8:11], a[196:199], a[132:135], v[8:11]\n"
    "ds_read_b128 a[220:223], %[kb1] offset:19456\n"
    "v_accvgpr_mov_b32 a2, a98\n"
    "v_mfma_f32_16x16x32_bf16 v[16:19], a[192:195], a[64:67], 0\n"
    "ds_read_b128 a[192:195], %[kb0] offset:24576\n"
    "v_accvgpr_mov_b32 a3, a99\n"
    "v_mfma_f32_16x16x32_bf16 v[16:19], a[196:199], a[68:71], v[16:19]\n"
    "buffer_load_dwordx4 %[vok], %[srk], s46 offen offset:128 nt lds\n"
    "v_mfma_f32_16x16x32_bf16 v[0:3], a[200:203], a[104:107], v[0:3]\n"
    "v_mul_f32 v78, v219, v78\n"
    "v_accvgpr_mov_b32 a4, a100\n"
    "v_mfma_f32_16x16x32_bf16 v[0:3], a[204:207], a[108:111], v[0:3]\n"
    "v_mul_f32 v79, v219, v79\n"
    "v_accvgpr_mov_b32 a5, a101\n"
    "v_mfma_f32_16x16x32_bf16 v[8:11], a[200:203], a[136:139], v[8:11]\n"
    "s_add_u32 m0, s42, 0x700\n"
    "v_accvgpr_mov_b32 a6, a102\n"
    "v_cvt_pk_bf16_f32 v76, v76, v77\n"
    "v_mfma_f32_16x16x32_bf16 v[8:11], a[204:207], a[140:143], v[8:11]\n"
    "s_add_u32 s47, s41, 0x2000\n"
    "ds_read_b128 a[196:199], %[kb1] offset:24576\n"
    "v_accvgpr_mov_b32 a7, a103\n"
    "v_mfma_f32_16x16x32_bf16 v[16:19], a[200:203], a[72:75], v[16:19]\n"
    "ds_read_b128 a[200:203], %[kb0] offset:25600\n"
    "v_cvt_pk_bf16_f32 v77, v78, v79\n"
    "v_mfma_f32_16x16x32_bf16 v[16:19], a[204:207], a[76:79], v[16:19]\n"
    "buffer_load_dwordx4 %[vok], %[srk], s46 offen offset:256 nt lds\n"
    "v_mfma_f32_16x16x32_bf16 v[0:3], a[208:211], a[112:115], v[0:3]\n"
    "s_waitcnt lgkmcnt(4)\n"
    "ds_write_b64 %[stw], v[76:77] offset:0\n"
    "v_mfma_f32_16x16x32_bf16 v[0:3], a[212:215], a[116:119], v[0:3]\n"
    "v_accvgpr_mov_b32 a8, a104\n"
    "v_accvgpr_mov_b32 a9, a105\n"
    "v_mfma_f32_16x16x32_bf16 v[8:11], a[208:211], a[144:147], v[8:11]\n"
    "s_add_u32 m0, s42, 0xa80\n"
    "v_accvgpr_mov_b32 a10, a106\n"
    "v_accvgpr_mov_b32 a11, a107\n"
    "v_mfma_f32_16x16x32_bf16 v[8:11], a[212:215], a[148:151], v[8:11]\n"
    "ds_read_b128 a[204:207], %[kb1] offset:25600\n"
    "v_accvgpr_mov_b32 a12, a108\n"
    "v_mfma_f32_16x16x32_bf16 v[16:19], a[208:211], a[80:83], v[16:19]\n"
    "ds_read_b128 a[208:211], %[kb0] offset:26624\n"
    "v_mul_f32 v84, v219, v84\n"
    "v_mfma_f32_16x16x32_bf16 v[16:19], a[212:215], a[84:87], v[16:19]\n"
    "buffer_load_dwordx4 %[vok], %[srk], s46 offen offset:384 nt lds\n"
    "v_mfma_f32_16x16x32_bf16 v[0:3], a[216:219], a[120:123], v[0:3]\n"
    "s_waitcnt lgkmcnt(5)\n"
    "v_accvgpr_mov_b32 a13, a109\n"
    "v_mul_f32 v85, v219, v85\n"
    "v_mfma_f32_16x16x32_bf16 v[0:3], a[220:223], a[124:127], v[0:3]\n"
    "v_accvgpr_mov_b32 a14, a110\n"
    "v_accvgpr_mov_b32 a15, a111\n"
    "v_mfma_f32_16x16x32_bf16 v[8:11], a[216:219], a[152:155], v[8:11]\n"
    "v_mul_f32 v86, v219, v86\n"
    "v_accvgpr_mov_b32 a16, a112\n"
    "v_mfma_f32_16x16x32_bf16 v[8:11], a[220:223], a[156:159], v[8:11]\n"
    "ds_read_b128 a[212:215], %[kb1] offset:26624\n"
    "v_mul_f32 v87, v219, v87\n"
    "v_mfma_f32_16x16x32_bf16 v[16:19], a[216:219], a[88:91], v[16:19]\n"
    "ds_read_b128 a[216:219], %[kb0] offset:27648\n"
    "v_accvgpr_mov_b32 a17, a113\n"
    "v_mfma_f32_16x16x32_bf16 v[16:19], a[220:223], a[92:95], v[16:19]\n"
    "buffer_store_dwordx4 a[240:243], %[vos], %[sro], s47 offen offset:0 sc0 sc1\n"
    "v_mfma_f32_16x16x32_bf16 v[4:7], a[192:195], a[96:99], 0\n"
    "s_waitcnt lgkmcnt(4)\n"
    "v_accvgpr_mov_b32 a18, a114\n"
    "v_cvt_pk_bf16_f32 v84, v84, v85\n"
    "v_mfma_f32_16x16x32_bf16 v[4:7], a[196:199], a[100:103], v[4:7]\n"
    "v_accvgpr_mov_b32 a19, a115\n"
    "v_cvt_pk_bf16_f32 v85, v86, v87\n"
    "v_mfma_f32_16x16x32_bf16 v[12:15], a[192:195], a[128:131], 0\n"
    "ds_write_b64 %[stw], v[84:85] offset:32\n"
    "v_mfma_f32_16x16x32_bf16 v[12:15], a[196:199], a[132:135], v[12:15]\n"
    "ds_read_b128 a[220:223], %[kb1] offset:27648\n"
    "v_accvgpr_mov_b32 a20, a116\n"
    "v_mfma_f32_16x16x32_bf16 v[20:23], a[192:195], a[64:67], 0\n"
    "s_waitcnt vmcnt(21)\n"
    "s_barrier\n"
    "ds_read_b128 a[192:195], %[kb0] offset:49152\n"
    "v_accvgpr_mov_b32 a21, a117\n"
    "v_mfma_f32_16x16x32_bf16 v[20:23], a[196:199], a[68:71], v[20:23]\n"
    "v_accvgpr_mov_b32 a22, a118\n"
    "v_accvgpr_mov_b32 a23, a119\n"
    "v_mfma_f32_16x16x32_bf16 v[4:7], a[200:203], a[104:107], v[4:7]\n"
    "s_waitcnt lgkmcnt(4)\n"
    "v_accvgpr_mov_b32 a24, a120\n"
    "v_mul_f32 v92, v219, v92\n"
    "v_mfma_f32_16x16x32_bf16 v[4:7], a[204:207], a[108:111], v[4:7]\n"
    "s_cbranch_vccnz .Lpad0_%=\n"
    ".Lret0_%=:\n"
    "s_add_u32 s46, s36, 0x18000\n"
    "v_accvgpr_mov_b32 a25, a121\n"
    "v_mul_f32 v93, v219, v93\n"
    "v_mfma_f32_16x16x32_bf16 v[12:15], a[200:203], a[136:139], v[12:15]\n"
    "s_add_u32 m0, s42, 0x1c000\n"
    "v_accvgpr_mov_b32 a26, a122\n"
    "v_accvgpr_mov_b32 a27, a123\n"
    "v_mfma_f32_16x16x32_bf16 v[12:15], a[204:207], a[140:143], v[12:15]\n"
    "buffer_load_dwordx4 %[vok], %[srk], s46 offen nt lds\n"
    "v_mfma_f32_16x16x32_bf16 v[20:23], a[200:203], a[72:75], v[20:23]\n"
    "s_add_u32 m0, s42, 0x1c380\n"
    "ds_read_b128 a[196:199], %[kb1] offset:49152\n"
    "ds_read_b128 a[200:203], %[kb0] offset:50176\n"
    "v_mfma_f32_16x16x32_bf16 v[20:23], a[204:207], a[76:79], v[20:23]\n"
    "v_mul_f32 v94, v219, v94\n"
    "v_accvgpr_mov_b32 a28, a124\n"
    "v_mfma_f32_16x16x32_bf16 v[4:7], a[208:211], a[112:115], v[4:7]\n"
    "v_mul_f32 v95, v219, v95\n"
    "v_accvgpr_mov_b32 a29, a125\n"
    "v_mfma_f32_16x16x32_bf16 v[4:7], a[212:215], a[116:119], v[4:7]\n"
    "buffer_load_dwordx4 %[vok], %[srk], s46 offen offset:128 nt lds\n"
    "v_mfma_f32_16x16x32_bf16 v[12:15], a[208:211], a[144:147], v[12:15]\n"
    "v_accvgpr_mov_b32 a30, a126\n"
    "v_cvt_pk_bf16_f32 v92, v92, v93\n"
    "v_mfma_f32_16x16x32_bf16 v[12:15], a[212:215], a[148:151], v[12:15]\n"
    "ds_read_b128 a[204:207], %[kb1] offset:50176\n"
    "v_accvgpr_mov_b32 a31, a127\n"
    "v_mfma_f32_16x16x32_bf16 v[20:23], a[208:211], a[80:83], v[20:23]\n"
    "ds_read_b128 a[208:211], %[kb0] offset:51200\n"
    "v_cvt_pk_bf16_f32 v93, v94, v95\n"
    "v_mfma_f32_16x16x32_bf16 v[20:23], a[212:215], a[84:87], v[20:23]\n"
    "s_waitcnt lgkmcnt(4)\n"
    "ds_write_b64 %[stw], v[92:93] offset:64\n"
    "v_mfma_f32_16x16x32_bf16 v[4:7], a[216:219], a[120:123], v[4:7]\n"
    "v_accvgpr_mov_b32 a32, a128\n"
    "v_accvgpr_mov_b32 a33, a129\n"
    "v_mfma_f32_16x16x32_bf16 v[4:7], a[220:223], a[124:127], v[4:7]\n"
    "v_max3_f32 v216, v0, v1, v2\n"
    "v_max_f32 v216, v216, v3\n"
    "v_mfma_f32_16x16x32_bf16 v[12:15], a[216:219], a[152:155], v[12:15]\n"
    "v_accvgpr_mov_b32 a34, a130\n"
    "v_max3_f32 v217, v8, v9, v10\n"
    "v_mfma_f32_16x16x32_bf16 v[12:15], a[220:223], a[156:159], v[12:15]\n"
    "ds_read_b128 a[212:215], %[kb1] offset:51200\n"
    "v_max_f32 v217, v217, v11\n"
    "v_mfma_f32_16x16x32_bf16 v[20:23], a[216:219], a[88:91], v[20:23]\n"
    "ds_read_b128 a[216:219], %[kb0] offset:52224\n"
    "v_accvgpr_mov_b32 a35, a131\n"
    "v_mfma_f32_16x16x32_bf16 v[20:23], a[220:223], a[92:95], v[20:23]\n"
    "v_accvgpr_mov_b32 a36, a132\n"
    "v_max3_f32 v218, v16, v17, v18\n"
    "v_mfma_f32_16x16x32_bf16 v[24:27], a[192:195], a[96:99], 0\n"
    "s_waitcnt lgkmcnt(5)\n"
    "buffer_store_dwordx4 a[244:247], %[vos], %[sro], s47 offen offset:2048 sc0 sc1\n"
    "v_mfma_f32_16x16x32_bf16 v[24:27], a[196:199], a[100:103], v[24:27]\n"
    "s_add_u32 m0, s42, 0x1c700\n"
    "v_max_f32 v218, v218, v19\n"
    "v_mul_f32 v100, v219, v100\n"
    "v_mfma_f32_16x16x32_bf16 v[32:35], a[192:195], a[128:131], 0\n"
    "v_accvgpr_mov_b32 a37, a133\n"
    "v_mul_f32 v101, v219, v101\n"
    "v_mfma_f32_16x16x32_bf16 v[32:35], a[196:199], a[132:135], v[32:35]\n"
    "ds_read_b128 a[220:223], %[kb1] offset:52224\n"
    "v_accvgpr_mov_b32 a38, a134\n"
    "v_mfma_f32_16x16x32_bf16 v[40:43], a[192:195], a[64:67], 0\n"
    "buffer_load_dwordx4 %[vok], %[srk], s46 offen offset:256 nt lds\n"
    "v_mfma_f32_16x16x32_bf16 v[40:43], a[196:199], a[68:71], v[40:43]\n"
    "ds_read_b128 a[192:195], %[kb0] offset:57344\n"
    "v_accvgpr_mov_b32 a39, a135\n"
    "v_mfma_f32_16x16x32_bf16 v[24:27], a[200:203], a[104:107], v[24:27]\n"
    "s_waitcnt lgkmcnt(4)\n"
    "v_mul_f32 v102, v219, v102\n"
    "v_accvgpr_mov_b32 a40, a136\n"
    "v_mfma_f32_16x16x32_bf16 v[24:27], a[204:207], a[108:111], v[24:27]\n"
    "s_add_u32 m0, s42, 0x1ca80\n"
    "v_mul_f32 v103, v219, v103\n"
    "v_accvgpr_mov_b32 a41, a137\n"
    "v_mfma_f32_16x16x32_bf16 v[32:35], a[200:203], a[136:139], v[32:35]\n"
    "v_accvgpr_mov_b32 a42, a138\n"
    "v_cvt_pk_bf16_f32 v100, v100, v101\n"
    "v_mfma_f32_16x16x32_bf16 v[32:35], a[204:207], a[140:143], v[32:35]\n"
    "ds_read_b128 a[196:199], %[kb1] offset:57344\n"
    "v_accvgpr_mov_b32 a43, a139\n"
    "v_mfma_f32_16x16x32_bf16 v[40:43], a[200:203], a[72:75], v[40:43]\n"
    "buffer_load_dwordx4 %[vok], %[srk], s46 offen offset:384 nt lds\n"
    "v_mfma_f32_16x16x32_bf16 v[40:43], a[204:207], a[76:79], v[40:43]\n"
    "ds_read_b128 a[200:203], %[kb0] offset:58368\n"
    "v_cvt_pk_bf16_f32 v101, v102, v103\n"
    "v_mfma_f32_16x16x32_bf16 v[24:27], a[208:211], a[112:115], v[24:27]\n"
    "s_waitcnt lgkmcnt(4)\n"
    "ds_write_b64 %[stw], v[100:101] offset:96\n"
    "v_mfma_f32_16x16x32_bf16 v[24:27], a[212:215], a[116:119], v[24:27]\n"
    "v_accvgpr_mov_b32 a44, a140\n"
    "v_accvgpr_mov_b32 a45, a141\n"
    "v_mfma_f32_16x16x32_bf16 v[32:35], a[208:211], a[144:147], v[32:35]\n"
    "v_accvgpr_mov_b32 a46, a142\n"
    "v_accvgpr_mov_b32 a47, a143\n"
    "v_mfma_f32_16x16x32_bf16 v[32:35], a[212:215], a[148:151], v[32:35]\n"
    "s_cbranch_vccnz .Lpad1_%=\n"
    ".Lret1_%=:\n"
    "ds_read_b128 a[204:207], %[kb1] offset:58368\n"
    "v_accvgpr_mov_b32 a48, a144\n"
    "v_mfma_f32_16x16x32_bf16 v[40:43], a[208:211], a[80:83], v[40:43]\n"
    "ds_read_b128 a[208:211], %[kb0] offset:59392\n"
    "v_mul_f32 v108, v219, v108\n"
    "v_mfma_f32_16x16x32_bf16 v[40:43], a[212:215], a[84:87], v[40:43]\n"
    "v_accvgpr_mov_b32 a49, a145\n"
    "v_mul_f32 v109, v219, v109\n"
    "v_mfma_f32_16x16x32_bf16 v[24:27], a[216:219], a[120:123], v[24:27]\n"
    "s_waitcnt lgkmcnt(5)\n"
    "v_max3_f32 v216, v216, v4, v5\n"
    "v_max3_f32 v216, v216, v6, v7\n"
    "v_mfma_f32_16x16x32_bf16 v[24:27], a[220:223], a[124:127], v[24:27]\n"
    "v_accvgpr_mov_b32 a50, a146\n"
    "v_accvgpr_mov_b32 a51, a147\n"
    "v_mfma_f32_16x16x32_bf16 v[32:35], a[216:219], a[152:155], v[32:35]\n"
    "v_mul_f32 v110, v219, v110\n"
    "v_max3_f32 v217, v217, v12, v13\n"
    "v_mfma_f32_16x16x32_bf16 v[32:35], a[220:223], a[156:159], v[32:35]\n"
    "ds_read_b128 a[212:215], %[kb1] offset:59392\n"
    "v_max3_f32 v217, v217, v14, v15\n"
    "v_mfma_f32_16x16x32_bf16 v[40:43], a[216:219], a[88:91], v[40:43]\n"
    "ds_read_b128 a[216:219], %[kb0] offset:60416\n"
    "v_accvgpr_mov_b32 a52, a148\n"
    "v_mfma_f32_16x16x32_bf16 v[40:43], a[220:223], a[92:95], v[40:43]\n"
    "v_mul_f32 v111, v219, v111\n"
    "v_max3_f32 v218, v218, v20, v21\n"
    "v_mfma_f32_16x16x32_bf16 v[28:31], a[192:195], a[96:99], 0\n"
    "s_waitcnt lgkmcnt(4)\n"
    "v_max3_f32 v218, v218, v22, v23\n"
    "v_accvgpr_mov_b32 a53, a149\n"
    "v_mfma_f32_16x16x32_bf16 v[28:31], a[196:199], a[100:103], v[28:31]\n"
    "v_accvgpr_mov_b32 a54, a150\n"
    "v_cvt_pk_bf16_f32 v108, v108, v109\n"
    "v_mfma_f32_16x16x32_bf16 v[36:39], a[192:195], a[128:131], 0\n"
    "s_add_u32 s47, s41, 0x3000\n"
    "v_accvgpr_mov_b32 a55, a151\n"
    "v_cvt_pk_bf16_f32 v109, v110, v111\n"
    "v_mfma_f32_16x16x32_bf16 v[36:39], a[196:199], a[132:135], v[36:39]\n"
    "ds_read_b128 a[220:223], %[kb1] offset:60416\n"
    "v_accvgpr_mov_b32 a56, a152\n"
    "v_mfma_f32_16x16x32_bf16 v[44:47], a[192:195], a[64:67], 0\n"
    "s_waitcnt vmcnt(22)\n"
    "s_barrier\n"
    "ds_read_b128 a[192:195], %[kb0] offset:32768\n"
    "v_accvgpr_mov_b32 a57, a153\n"
    "v_mfma_f32_16x16x32_bf16 v[44:47], a[196:199], a[68:71], v[44:47]\n"
    "ds_write_b64 %[stw], v[108:109] offset:128\n"
    "v_mfma_f32_16x16x32_bf16 v[28:31], a[200:203], a[104:107], v[28:31]\n"
    "s_waitcnt lgkmcnt(4)\n"
    "v_accvgpr_mov_b32 a58, a154\n"
    "v_accvgpr_mov_b32 a59, a155\n"
    "v_mfma_f32_16x16x32_bf16 v[28:31], a[204:207], a[108:111], v[28:31]\n"
    "s_add_u32 s46, s36, 0x1c000\n"
    "v_mul_f32 v116, v219, v116\n"
    "v_accvgpr_mov_b32 a60, a156\n"
    "v_mfma_f32_16x16x32_bf16 v[36:39], a[200:203], a[136:139], v[36:39]\n"
    "s_add_u32 m0, s42, 0x4000\n"
    "v_accvgpr_mov_b32 a61, a157\n"
    "v_mul_f32 v117, v219, v117\n"
    "v_mfma_f32_16x16x32_bf16 v[36:39], a[204:207], a[140:143], v[36:39]\n"
    "buffer_load_dwordx4 %[vok], %[srk], s46 offen nt lds\n"
    "v_mfma_f32_16x16x32_bf16 v[44:47], a[200:203], a[72:75], v[44:47]\n"
    "s_add_u32 m0, s42, 0x4380\n"
    "ds_read_b128 a[196:199], %[kb1] offset:32768\n"
    "ds_read_b128 a[200:203], %[kb0] offset:33792\n"
    "v_mfma_f32_16x16x32_bf16 v[44:47], a[204:207], a[76:79], v[44:47]\n"
    "v_accvgpr_mov_b32 a62, a158\n"
    "v_mul_f32 v118, v219, v118\n"
    "v_mfma_f32_16x16x32_bf16 v[28:31], a[208:211], a[112:115], v[28:31]\n"
    "v_accvgpr_mov_b32 a63, a159\n"
    "v_mul_f32 v119, v219, v119\n"
    "v_mfma_f32_16x16x32_bf16 v[28:31], a[212:215], a[116:119], v[28:31]\n"
    "buffer_load_dwordx4 %[vok], %[srk], s46 offen offset:128 nt lds\n"
    "v_mfma_f32_16x16x32_bf16 v[36:39], a[208:211], a[144:147], v[36:39]\n"
    "v_cvt_pk_bf16_f32 v116, v116, v117\n"
    "v_max3_f32 v216, v216, v24, v25\n"
    "v_mfma_f32_16x16x32_bf16 v[36:39], a[212:215], a[148:151], v[36:39]\n"
    "ds_read_b128 a[204:207], %[kb1] offset:33792\n"
    "v_max3_f32 v216, v216, v26, v27\n"
    "v_mfma_f32_16x16x32_bf16 v[44:47], a[208:211], a[80:83], v[44:47]\n"
    "s_add_u32 m0, s42, 0x4700\n"
    "ds_read_b128 a[208:211], %[kb0] offset:34816\n"
    "v_cvt_pk_bf16_f32 v117, v118, v119\n"
    "v_mfma_f32_16x16x32_bf16 v[44:47], a[212:215], a[84:87], v[44:47]\n"
    "s_waitcnt lgkmcnt(4)\n"
    "ds_write_b64 %[stw], v[116:117] offset:160\n"
    "v_mfma_f32_16x16x32_bf16 v[28:31], a[216:219], a[120:123], v[28:31]\n"
    "buffer_store_dwordx4 a[248:251], %[vos], %[sro], s47 offen offset:0 sc0 sc1\n"
    "v_mfma_f32_16x16x32_bf16 v[28:31], a[220:223], a[124:127], v[28:31]\n"
    "buffer_load_dwordx4 %[vok], %[srk], s46 offen offset:256 nt lds\n"
    "v_mfma_f32_16x16x32_bf16 v[36:39], a[216:219], a[152:155], v[36:39]\n"
    "v_max3_f32 v217, v217, v32, v33\n"
    "v_max3_f32 v217, v217, v34, v35\n"
    "v_mfma_f32_16x16x32_bf16 v[36:39], a[220:223], a[156:159], v[36:39]\n"
    "ds_read_b128 a[212:215], %[kb1] offset:34816\n"
    "v_max3_f32 v218, v218, v40, v41\n"
    "v_mfma_f32_16x16x32_bf16 v[44:47], a[216:219], a[88:91], v[44:47]\n"
    "s_add_u32 m0, s42, 0x4a80\n"
    "ds_read_b128 a[216:219], %[kb0] offset:35840\n"
    "v_max3_f32 v218, v218, v42, v43\n"
    "v_mfma_f32_16x16x32_bf16 v[44:47], a[220:223], a[92:95], v[44:47]\n"
    "s_cbranch_vccnz .Lpad2_%=\n"
    ".Lret2_%=:\n"
    "v_mul_f32 v124, v219, v124\n"
    "v_mul_f32 v125, v219, v125\n"
    "v_mfma_f32_16x16x32_bf16 a[96:99], a[192:195], a[0:3], 0\n"
    "s_waitcnt lgkmcnt(5)\n"
    "v_mul_f32 v126, v219, v126\n"
    "v_mul_f32 v127, v219, v127\n"
    "v_mfma_f32_16x16x32_bf16 a[96:99], a[196:199], a[4:7], a[96:99]\n"
    "buffer_load_dwordx4 %[vok], %[srk], s46 offen offset:384 nt lds\n"
    "v_mfma_f32_16x16x32_bf16 a[104:107], a[192:195], a[32:35], 0\n"
    "v_cvt_pk_bf16_f32 v124, v124, v125\n"
    "v_cvt_pk_bf16_f32 v125, v126, v127\n"
    "v_mfma_f32_16x16x32_bf16 a[104:107], a[196:199], a[36:39], a[104:107]\n"
    "ds_read_b128 a[220:223], %[kb1] offset:35840\n"
    "v_max3_f32 v216, v216, v28, v29\n"
    "v_mfma_f32_16x16x32_bf16 a[112:115], a[192:195], a[64:67], 0\n"
    "ds_read_b128 a[192:195], %[kb0] offset:40960\n"
    "v_max3_f32 v216, v216, v30, v31\n"
    "v_mfma_f32_16x16x32_bf16 a[112:115], a[196:199], a[68:71], a[112:115]\n"
    "ds_write_b64 %[stw], v[124:125] offset:192\n"
    "v_mfma_f32_16x16x32_bf16 a[96:99], a[200:203], a[8:11], a[96:99]\n"
    "s_waitcnt lgkmcnt(5)\n"
    "v_mul_f32 v132, v219, v132\n"
    "v_max3_f32 v217, v217, v36, v37\n"
    "v_mfma_f32_16x16x32_bf16 a[96:99], a[204:207], a[12:15], a[96:99]\n"
    "v_max3_f32 v217, v217, v38, v39\n"
    "v_mul_f32 v133, v219, v133\n"
    "v_mfma_f32_16x16x32_bf16 a[104:107], a[200:203], a[40:43], a[104:107]\n"
    "v_max3_f32 v218, v218, v44, v45\n"
    "v_max3_f32 v218, v218, v46, v47\n"
    "v_mfma_f32_16x16x32_bf16 a[104:107], a[204:207], a[44:47], a[104:107]\n"
    "ds_read_b128 a[196:199], %[kb1] offset:40960\n"
    "v_mul_f32 v134, v219, v134\n"
    "v_mfma_f32_16x16x32_bf16 a[112:115], a[200:203], a[72:75], a[112:115]\n"
    "ds_read_b128 a[200:203], %[kb0] offset:41984\n"
    "v_mul_f32 v135, v219, v135\n"
    "v_mfma_f32_16x16x32_bf16 a[112:115], a[204:207], a[76:79], a[112:115]\n"
    "v_cvt_pk_bf16_f32 v132, v132, v133\n"
    "v_mfma_f32_16x16x32_bf16 a[96:99], a[208:211], a[16:19], a[96:99]\n"
    "s_waitcnt lgkmcnt(5)\n"
    "v_mfma_f32_16x16x32_bf16 a[96:99], a[212:215], a[20:23], a[96:99]\n"
    "v_mfma_f32_16x16x32_bf16 a[104:107], a[208:211], a[48:51], a[104:107]\n"
    "v_cvt_pk_bf16_f32 v133, v134, v135\n"
    "v_mfma_f32_16x16x32_bf16 a[104:107], a[212:215], a[52:55], a[104:107]\n"
    "ds_read_b128 a[204:207], %[kb1] offset:41984\n"
    "v_mfma_f32_16x16x32_bf16 a[112:115], a[208:211], a[80:83], a[112:115]\n"
    "ds_read_b128 a[208:211], %[kb0] offset:43008\n"
    "v_mfma_f32_16x16x32_bf16 a[112:115], a[212:215], a[84:87], a[112:115]\n"
    "v_mfma_f32_16x16x32_bf16 a[96:99], a[216:219], a[24:27], a[96:99]\n"
    "s_waitcnt lgkmcnt(4)\n"
    "ds_write_b64 %[stw], v[132:133] offset:224\n"
    "v_mfma_f32_16x16x32_bf16 a[96:99], a[220:223], a[28:31], a[96:99]\n"
    "buffer_store_dwordx4 a[252:255], %[vos], %[sro], s47 offen offset:2048 sc0 sc1\n"
    "v_mfma_f32_16x16x32_bf16 a[104:107], a[216:219], a[56:59], a[104:107]\n"
    "ds_read_b128 a[240:243], %[str] offset:0\n"
    "v_mfma_f32_16x16x32_bf16 a[104:107], a[220:223], a[60:63], a[104:107]\n"
    "ds_read_b128 a[212:215], %[kb1] offset:43008\n"
    "v_mfma_f32_16x16x32_bf16 a[112:115], a[216:219], a[88:91], a[112:115]\n"
    "ds_read_b128 a[216:219], %[kb0] offset:44032\n"
    "v_mfma_f32_16x16x32_bf16 a[112:115], a[220:223], a[92:95], a[112:115]\n"
    "ds_read_b128 a[244:247], %[str] offset:1088\n"
    "v_mfma_f32_16x16x32_bf16 a[100:103], a[192:195], a[0:3], 0\n"
    "s_waitcnt lgkmcnt(7)\n"
    "ds_read_b128 a[248:251], %[str] offset:2176\n"
    "v_accvgpr_read_b32 v230, a96\n"
    "v_mfma_f32_16x16x32_bf16 a[100:103], a[196:199], a[4:7], a[100:103]\n"
    "v_accvgpr_read_b32 v231, a97\n"
    "v_accvgpr_read_b32 v232, a98\n"
    "v_mfma_f32_16x16x32_bf16 a[108:111], a[192:195], a[32:35], 0\n"
    "ds_read_b128 a[252:255], %[str] offset:3264\n"
    "v_accvgpr_read_b32 v233, a99\n"
    "v_mfma_f32_16x16x32_bf16 a[108:111], a[196:199], a[36:39], a[108:111]\n"
    "ds_read_b128 a[220:223], %[kb1] offset:44032\n"
    "v_max3_f32 v216, v216, v230, v231\n"
    "v_mfma_f32_16x16x32_bf16 a[116:119], a[192:195], a[64:67], 0\n"
    "s_waitcnt vmcnt(16)\n"
    "s_barrier\n"
    "v_add_u32 %[kb0], 0x10000, %[kb0]\n"
    "v_add_u32 %[kb1], 0x10000, %[kb1]\n"
    "v_mfma_f32_16x16x32_bf16 a[116:119], a[196:199], a[68:71], a[116:119]\n"
    "ds_read_b128 a[192:195], %[kb0] offset:32768\n"
    "v_max3_f32 v216, v216, v232, v233\n"
    "v_mfma_f32_16x16x32_bf16 a[100:103], a[200:203], a[8:11], a[100:103]\n"
    "s_waitcnt lgkmcnt(6)\n"
    "v_accvgpr_read_b32 v234, a104\n"
    "v_accvgpr_read_b32 v235, a105\n"
    "v_mfma_f32_16x16x32_bf16 a[100:103], a[204:207], a[12:15], a[100:103]\n"
    "s_cbranch_vccnz .Lpad3_%=\n"
    ".Lret3_%=:\n"
    "s_add_u32 s46, s36, 0x20000\n"
    "v_accvgpr_read_b32 v236, a106\n"
    "v_accvgpr_read_b32 v237, a107\n"
    "v_mfma_f32_16x16x32_bf16 a[108:111], a[200:203], a[40:43], a[108:111]\n"
    "s_add_u32 m0, s42, 0xc000\n"
    "v_max3_f32 v217, v217, v234, v235\n"
    "v_max3_f32 v217, v217, v236, v237\n"
    "v_mfma_f32_16x16x32_bf16 a[108:111], a[204:207], a[44:47], a[108:111]\n"
    "buffer_load_dwordx4 %[vok], %[srk], s46 offen nt lds\n"
    "v_mfma_f32_16x16x32_bf16 a[116:119], a[200:203], a[72:75], a[116:119]\n"
    "s_add_u32 m0, s42, 0xc380\n"
    "ds_read_b128 a[196:199], %[kb1] offset:32768\n"
    "ds_read_b128 a[200:203], %[kb0] offset:33792\n"
    "v_mfma_f32_16x16x32_bf16 a[116:119], a[204:207], a[76:79], a[116:119]\n"
    "v_accvgpr_read_b32 v230, a112\n"
    "v_accvgpr_read_b32 v231, a113\n"
    "v_mfma_f32_16x16x32_bf16 a[100:103], a[208:211], a[16:19], a[100:103]\n"
    "v_accvgpr_read_b32 v232, a114\n"
    "v_accvgpr_read_b32 v233, a115\n"
    "v_mfma_f32_16x16x32_bf16 a[100:103], a[212:215], a[20:23], a[100:103]\n"
    "buffer_load_dwordx4 %[vok], %[srk], s46 offen offset:128 nt lds\n"
    "v_mfma_f32_16x16x32_bf16 a[108:111], a[208:211], a[48:51], a[108:111]\n"
    "v_max3_f32 v218, v218, v230, v231\n"
    "v_max3_f32 v218, v218, v232, v233\n"
    "v_mfma_f32_16x16x32_bf16 a[108:111], a[212:215], a[52:55], a[108:111]\n"
    "ds_read_b128 a[204:207], %[kb1] offset:33792\n"
    "v_mfma_f32_16x16x32_bf16 a[116:119], a[208:211], a[80:83], a[116:119]\n"
    "ds_read_b128 a[208:211], %[kb0] offset:34816\n"
    "v_mfma_f32_16x16x32_bf16 a[116:119], a[212:215], a[84:87], a[116:119]\n"
    "s_waitcnt lgkmcnt(4)\n"
    "v_mfma_f32_16x16x32_bf16 a[100:103], a[216:219], a[24:27], a[100:103]\n"
    "v_mfma_f32_16x16x32_bf16 a[100:103], a[220:223], a[28:31], a[100:103]\n"
    "v_mfma_f32_16x16x32_bf16 a[108:111], a[216:219], a[56:59], a[108:111]\n"
    "v_mfma_f32_16x16x32_bf16 a[108:111], a[220:223], a[60:63], a[108:111]\n"
    "s_add_u32 m0, s42, 0xc700\n"
    "ds_read_b128 a[212:215], %[kb1] offset:34816\n"
    "v_mfma_f32_16x16x32_bf16 a[116:119], a[216:219], a[88:91], a[116:119]\n"
    "ds_read_b128 a[216:219], %[kb0] offset:35840\n"
    "v_mfma_f32_16x16x32_bf16 a[116:119], a[220:223], a[92:95], a[116:119]\n"
    "v_mfma_f32_16x16x32_bf16 a[120:123], a[192:195], a[0:3], 0\n"
    "s_waitcnt lgkmcnt(4)\n"
    "buffer_load_dwordx4 %[vok], %[srk], s46 offen offset:256 nt lds\n"
    "v_mfma_f32_16x16x32_bf16 a[120:123], a[196:199], a[4:7], a[120:123]\n"
    "v_accvgpr_read_b32 v234, a100\n"
    "v_accvgpr_read_b32 v235, a101\n"
    "v_mfma_f32_16x16x32_bf16 a[128:131], a[192:195], a[32:35], 0\n"
    "v_mul_f32 v140, v220, v140\n"
    "v_mul_f32 v141, v220, v141\n"
    "v_mfma_f32_16x16x32_bf16 a[128:131], a[196:199], a[36:39], a[128:131]\n"
    "s_add_u32 m0, s42, 0xca80\n"
    "ds_read_b128 a[220:223], %[kb1] offset:35840\n"
    "v_mul_f32 v142, v220, v142\n"
    "v_mfma_f32_16x16x32_bf16 a[136:139], a[192:195], a[64:67], 0\n"
    "ds_read_b128 a[192:195], %[kb0] offset:40960\n"
    "v_mul_f32 v143, v220, v143\n"
    "v_mfma_f32_16x16x32_bf16 a[136:139], a[196:199], a[68:71], a[136:139]\n"
    "v_cvt_pk_bf16_f32 v140, v140, v141\n"
    "v_cvt_pk_bf16_f32 v141, v142, v143\n"
    "v_mfma_f32_16x16x32_bf16 a[120:123], a[200:203], a[8:11], a[120:123]\n"
    "s_waitcnt lgkmcnt(4)\n"
    "buffer_load_dwordx4 %[vok], %[srk], s46 offen offset:384 nt lds\n"
    "v_mfma_f32_16x16x32_bf16 a[120:123], a[204:207], a[12:15], a[120:123]\n"
    "ds_write_b64 %[stw], v[140:141] offset:0\n"
    "v_mfma_f32_16x16x32_bf16 a[128:131], a[200:203], a[40:43], a[128:131]\n"
    "s_add_u32 s47, s41, 0x0\n"
    "v_accvgpr_read_b32 v236, a102\n"
    "v_accvgpr_read_b32 v237, a103\n"
    "v_mfma_f32_16x16x32_bf16 a[128:131], a[204:207], a[44:47], a[128:131]\n"
    "ds_read_b128 a[196:199], %[kb1] offset:40960\n"
    "v_max3_f32 v216, v216, v234, v235\n"
    "v_mfma_f32_16x16x32_bf16 a[136:139], a[200:203], a[72:75], a[136:139]\n"
    "ds_read_b128 a[200:203], %[kb0] offset:41984\n"
    "v_max3_f32 v216, v216, v236, v237\n"
    "v_mfma_f32_16x16x32_bf16 a[136:139], a[204:207], a[76:79], a[136:139]\n"
    "v_mul_f32 v148, v220, v148\n"
    "v_accvgpr_read_b32 v230, a108\n"
    "v_mfma_f32_16x16x32_bf16 a[120:123], a[208:211], a[16:19], a[120:123]\n"
    "s_waitcnt lgkmcnt(5)\n"
    "v_accvgpr_read_b32 v231, a109\n"
    "v_accvgpr_read_b32 v232, a110\n"
    "v_mfma_f32_16x16x32_bf16 a[120:123], a[212:215], a[20:23], a[120:123]\n"
    "v_accvgpr_read_b32 v233, a111\n"
    "v_max3_f32 v217, v217, v230, v231\n"
    "v_mfma_f32_16x16x32_bf16 a[128:131], a[208:211], a[48:51], a[128:131]\n"
    "v_max3_f32 v217, v217, v232, v233\n"
    "v_mul_f32 v149, v220, v149\n"
    "v_mfma_f32_16x16x32_bf16 a[128:131], a[212:215], a[52:55], a[128:131]\n"
    "s_cbranch_vccnz .Lpad4_%=\n"
    ".Lret4_%=:\n"
    "ds_read_b128 a[204:207], %[kb1] offset:41984\n"
    "v_accvgpr_read_b32 v234, a116\n"
    "v_mfma_f32_16x16x32_bf16 a[136:139], a[208:211], a[80:83], a[136:139]\n"
    "ds_read_b128 a[208:211], %[kb0] offset:43008\n"
    "v_accvgpr_read_b32 v235, a117\n"
    "v_mfma_f32_16x16x32_bf16 a[136:139], a[212:215], a[84:87], a[136:139]\n"
    "v_accvgpr_read_b32 v236, a118\n"
    "v_accvgpr_read_b32 v237, a119\n"
    "v_mfma_f32_16x16x32_bf16 a[120:123], a[216:219], a[24:27], a[120:123]\n"
    "s_waitcnt lgkmcnt(4)\n"
    "v_max3_f32 v218, v218, v234, v235\n"
    "v_max3_f32 v218, v218, v236, v237\n"
    "v_mfma_f32_16x16x32_bf16 a[120:123], a[220:223], a[28:31], a[120:123]\n"
    "v_mul_f32 v150, v220, v150\n"
    "v_mul_f32 v151, v220, v151\n"
    "v_mfma_f32_16x16x32_bf16 a[128:131], a[216:219], a[56:59], a[128:131]\n"
    "v_cvt_pk_bf16_f32 v148, v148, v149\n"
    "v_cvt_pk_bf16_f32 v149, v150, v151\n"
    "v_mfma_f32_16x16x32_bf16 a[128:131], a[220:223], a[60:63], a[128:131]\n"
    "ds_read_b128 a[212:215], %[kb1] offset:43008\n"
    "v_mfma_f32_16x16x32_bf16 a[136:139], a[216:219], a[88:91], a[136:139]\n"
    "ds_read_b128 a[216:219], %[kb0] offset:44032\n"
    "v_mfma_f32_16x16x32_bf16 a[136:139], a[220:223], a[92:95], a[136:139]\n"
    "ds_write_b64 %[stw], v[148:149] offset:32\n"
    "v_mfma_f32_16x16x32_bf16 a[124:127], a[192:195], a[0:3], 0\n"
    "s_waitcnt lgkmcnt(5)\n"
    "v_mul_f32 v156, v220, v156\n"
    "v_accvgpr_read_b32 v230, a120\n"
    "v_mfma_f32_16x16x32_bf16 a[124:127], a[196:199], a[4:7], a[124:127]\n"
    "buffer_store_dwordx4 a[240:243], %[vos], %[sro], s47 offen offset:256 sc0 sc1\n"
    "v_mfma_f32_16x16x32_bf16 a[132:135], a[192:195], a[32:35], 0\n"
    "v_mul_f32 v157, v220, v157\n"
    "v_accvgpr_read_b32 v231, a121\n"
    "v_mfma_f32_16x16x32_bf16 a[132:135], a[196:199], a[36:39], a[132:135]\n"
    "ds_read_b128 a[220:223], %[kb1] offset:44032\n"
    "v_accvgpr_read_b32 v232, a122\n"
    "v_mfma_f32_16x16x32_bf16 a[140:143], a[192:195], a[64:67], 0\n"
    "s_waitcnt vmcnt(21)\n"
    "s_barrier\n"
    "ds_read_b128 a[192:195], %[kb0] offset:16384\n"
    "v_accvgpr_read_b32 v233, a123\n"
    "v_mfma_f32_16x16x32_bf16 a[140:143], a[196:199], a[68:71], a[140:143]\n"
    "v_max3_f32 v216, v216, v230, v231\n"
    "v_max3_f32 v216, v216, v232, v233\n"
    "v_mfma_f32_16x16x32_bf16 a[124:127], a[200:203], a[8:11], a[124:127]\n"
    "s_waitcnt lgkmcnt(4)\n"
    "v_accvgpr_read_b32 v234, a128\n"
    "v_accvgpr_read_b32 v235, a129\n"
    "v_mfma_f32_16x16x32_bf16 a[124:127], a[204:207], a[12:15], a[124:127]\n"
    "s_add_u32 s46, s36, 0x24000\n"
    "v_accvgpr_read_b32 v236, a130\n"
    "v_accvgpr_read_b32 v237, a131\n"
    "v_mfma_f32_16x16x32_bf16 a[132:135], a[200:203], a[40:43], a[132:135]\n"
    "s_add_u32 m0, s42, 0x8000\n"
    "v_max3_f32 v217, v217, v234, v235\n"
    "v_max3_f32 v217, v217, v236, v237\n"
    "v_mfma_f32_16x16x32_bf16 a[132:135], a[204:207], a[44:47], a[132:135]\n"
    "buffer_load_dwordx4 %[vok], %[srk], s46 offen nt lds\n"
    "v_mfma_f32_16x16x32_bf16 a[140:143], a[200:203], a[72:75], a[140:143]\n"
    "s_add_u32 m0, s42, 0x8380\n"
    "ds_read_b128 a[196:199], %[kb1] offset:16384\n"
    "ds_read_b128 a[200:203], %[kb0] offset:17408\n"
    "v_mfma_f32_16x16x32_bf16 a[140:143], a[204:207], a[76:79], a[140:143]\n"
    "v_mul_f32 v158, v220, v158\n"
    "v_accvgpr_read_b32 v230, a136\n"
    "v_mfma_f32_16x16x32_bf16 a[124:127], a[208:211], a[16:19], a[124:127]\n"
    "v_accvgpr_read_b32 v231, a137\n"
    "v_accvgpr_read_b32 v232, a138\n"
    "v_mfma_f32_16x16x32_bf16 a[124:127], a[212:215], a[20:23], a[124:127]\n"
    "buffer_load_dwordx4 %[vok], %[srk], s46 offen offset:128 nt lds\n"
    "v_mfma_f32_16x16x32_bf16 a[132:135], a[208:211], a[48:51], a[132:135]\n"
    "v_accvgpr_read_b32 v233, a139\n"
    "v_max3_f32 v218, v218, v230, v231\n"
    "v_mfma_f32_16x16x32_bf16 a[132:135], a[212:215], a[52:55], a[132:135]\n"
    "ds_read_b128 a[204:207], %[kb1] offset:17408\n"
    "v_max3_f32 v218, v218, v232, v233\n"
    "v_mfma_f32_16x16x32_bf16 a[140:143], a[208:211], a[80:83], a[140:143]\n"
    "s_add_u32 m0, s42, 0x8700\n"
    "ds_read_b128 a[208:211], %[kb0] offset:18432\n"
    "v_mul_f32 v159, v220, v159\n"
    "v_mfma_f32_16x16x32_bf16 a[140:143], a[212:215], a[84:87], a[140:143]\n"
    "s_waitcnt lgkmcnt(4)\n"
    "v_cvt_pk_bf16_f32 v156, v156, v157\n"
    "v_cvt_pk_bf16_f32 v157, v158, v159\n"
    "v_mfma_f32_16x16x32_bf16 a[124:127], a[216:219], a[24:27], a[124:127]\n"
    "ds_write_b64 %[stw], v[156:157] offset:64\n"
    "v_mfma_f32_16x16x32_bf16 a[124:127], a[220:223], a[28:31], a[124:127]\n"
    "buffer_load_dwordx4 %[vok], %[srk], s46 offen offset:256 nt lds\n"
    "v_mfma_f32_16x16x32_bf16 a[132:135], a[216:219], a[56:59], a[132:135]\n"
    "v_mul_f32 v164, v220, v164\n"
    "v_mfma_f32_16x16x32_bf16 a[132:135], a[220:223], a[60:63], a[132:135]\n"
    "ds_read_b128 a[212:215], %[kb1] offset:18432\n"
    "v_mfma_f32_16x16x32_bf16 a[140:143], a[216:219], a[88:91], a[140:143]\n"
    "s_add_u32 m0, s42, 0x8a80\n"
    "ds_read_b128 a[216:219], %[kb0] offset:19456\n"
    "v_mul_f32 v165, v220, v165\n"
    "v_mfma_f32_16x16x32_bf16 a[140:143], a[220:223], a[92:95], a[140:143]\n"
    "s_cbranch_vccnz .Lpad5_%=\n"
    ".Lret5_%=:\n"
    "v_mfma_f32_16x16x32_bf16 a[144:147], a[192:195], a[0:3], 0\n"
    "s_waitcnt lgkmcnt(5)\n"
    "v_mul_f32 v166, v220, v166\n"
    "v_accvgpr_read_b32 v234, a124\n"
    "v_mfma_f32_16x16x32_bf16 a[144:147], a[196:199], a[4:7], a[144:147]\n"
    "buffer_load_dwordx4 %[vok], %[srk], s46 offen offset:384 nt lds\n"
    "v_mfma_f32_16x16x32_bf16 a[152:155], a[192:195], a[32:35], 0\n"
    "v_accvgpr_read_b32 v235, a125\n"
    "v_accvgpr_read_b32 v236, a126\n"
    "v_mfma_f32_16x16x32_bf16 a[152:155], a[196:199], a[36:39], a[152:155]\n"
    "ds_read_b128 a[220:223], %[kb1] offset:19456\n"
    "v_accvgpr_read_b32 v237, a127\n"
    "v_mfma_f32_16x16x32_bf16 a[160:163], a[192:195], a[64:67], 0\n"
    "ds_read_b128 a[192:195], %[kb0] offset:24576\n"
    "v_max3_f32 v216, v216, v234, v235\n"
    "v_mfma_f32_16x16x32_bf16 a[160:163], a[196:199], a[68:71], a[160:163]\n"
    "v_max3_f32 v216, v216, v236, v237\n"
    "v_mul_f32 v167, v220, v167\n"
    "v_mfma_f32_16x16x32_bf16 a[144:147], a[200:203], a[8:11], a[144:147]\n"
    "s_waitcnt lgkmcnt(4)\n"
    "v_accvgpr_read_b32 v230, a132\n"
    "v_accvgpr_read_b32 v231, a133\n"
    "v_mfma_f32_16x16x32_bf16 a[144:147], a[204:207], a[12:15], a[144:147]\n"
    "v_accvgpr_read_b32 v232, a134\n"
    "v_accvgpr_read_b32 v233, a135\n"
    "v_mfma_f32_16x16x32_bf16 a[152:155], a[200:203], a[40:43], a[152:155]\n"
    "v_max3_f32 v217, v217, v230, v231\n"
    "v_max3_f32 v217, v217, v232, v233\n"
    "v_mfma_f32_16x16x32_bf16 a[152:155], a[204:207], a[44:47], a[152:155]\n"
    "ds_read_b128 a[196:199], %[kb1] offset:24576\n"
    "v_accvgpr_read_b32 v234, a140\n"
    "v_mfma_f32_16x16x32_bf16 a[160:163], a[200:203], a[72:75], a[160:163]\n"
    "ds_read_b128 a[200:203], %[kb0] offset:25600\n"
    "v_accvgpr_read_b32 v235, a141\n"
    "v_mfma_f32_16x16x32_bf16 a[160:163], a[204:207], a[76:79], a[160:163]\n"
    "buffer_store_dwordx4 a[244:247], %[vos], %[sro], s47 offen offset:2304 sc0 sc1\n"
    "v_mfma_f32_16x16x32_bf16 a[144:147], a[208:211], a[16:19], a[144:147]\n"
    "s_waitcnt lgkmcnt(4)\n"
    "v_accvgpr_read_b32 v236, a142\n"
    "v_accvgpr_read_b32 v237, a143\n"
    "v_mfma_f32_16x16x32_bf16 a[144:147], a[212:215], a[20:23], a[144:147]\n"
    "v_max3_f32 v218, v218, v234, v235\n"
    "v_max3_f32 v218, v218, v236, v237\n"
    "v_mfma_f32_16x16x32_bf16 a[152:155], a[208:211], a[48:51], a[152:155]\n"
    "v_cvt_pk_bf16_f32 v164, v164, v165\n"
    "v_cvt_pk_bf16_f32 v165, v166, v167\n"
    "v_mfma_f32_16x16x32_bf16 a[152:155], a[212:215], a[52:55], a[152:155]\n"
    "ds_read_b128 a[204:207], %[kb1] offset:25600\n"
    "v_mfma_f32_16x16x32_bf16 a[160:163], a[208:211], a[80:83], a[160:163]\n"
    "ds_read_b128 a[208:211], %[kb0] offset:26624\n"
    "v_mfma_f32_16x16x32_bf16 a[160:163], a[212:215], a[84:87], a[160:163]\n"
    "ds_write_b64 %[stw], v[164:165] offset:96\n"
    "v_mfma_f32_16x16x32_bf16 a[144:147], a[216:219], a[24:27], a[144:147]\n"
    "s_waitcnt lgkmcnt(5)\n"
    "v_mul_f32 v172, v220, v172\n"
    "v_mul_f32 v173, v220, v173\n"
    "v_mfma_f32_16x16x32_bf16 a[144:147], a[220:223], a[28:31], a[144:147]\n"
    "v_mfma_f32_16x16x32_bf16 a[152:155], a[216:219], a[56:59], a[152:155]\n"
    "v_mfma_f32_16x16x32_bf16 a[152:155], a[220:223], a[60:63], a[152:155]\n"
    "ds_read_b128 a[212:215], %[kb1] offset:26624\n"
    "v_mul_f32 v174, v220, v174\n"
    "v_mfma_f32_16x16x32_bf16 a[160:163], a[216:219], a[88:91], a[160:163]\n"
    "ds_read_b128 a[216:219], %[kb0] offset:27648\n"
    "v_mfma_f32_16x16x32_bf16 a[160:163], a[220:223], a[92:95], a[160:163]\n"
    "v_mfma_f32_16x16x32_bf16 a[148:151], a[192:195], a[0:3], 0\n"
    "s_waitcnt lgkmcnt(5)\n"
    "v_mul_f32 v175, v220, v175\n"
    "v_accvgpr_read_b32 v230, a144\n"
    "v_mfma_f32_16x16x32_bf16 a[148:151], a[196:199], a[4:7], a[148:151]\n"
    "v_accvgpr_read_b32 v231, a145\n"
    "v_accvgpr_read_b32 v232, a146\n"
    "v_mfma_f32_16x16x32_bf16 a[156:159], a[192:195], a[32:35], 0\n"
    "v_accvgpr_read_b32 v233, a147\n"
    "v_max3_f32 v216, v216, v230, v231\n"
    "v_mfma_f32_16x16x32_bf16 a[156:159], a[196:199], a[36:39], a[156:159]\n"
    "ds_read_b128 a[220:223], %[kb1] offset:27648\n"
    "v_max3_f32 v216, v216, v232, v233\n"
    "v_mfma_f32_16x16x32_bf16 a[164:167], a[192:195], a[64:67], 0\n"
    "s_waitcnt vmcnt(26)\n"
    "s_barrier\n"
    "ds_read_b128 a[192:195], %[kb0] offset:0\n"
    "v_cvt_pk_bf16_f32 v172, v172, v173\n"
    "v_mfma_f32_16x16x32_bf16 a[164:167], a[196:199], a[68:71], a[164:167]\n"
    "v_accvgpr_read_b32 v234, a152\n"
    "v_accvgpr_read_b32 v235, a153\n"
    "v_mfma_f32_16x16x32_bf16 a[148:151], a[200:203], a[8:11], a[148:151]\n"
    "s_waitcnt lgkmcnt(3)\n"
    "v_accvgpr_read_b32 v236, a154\n"
    "v_accvgpr_read_b32 v237, a155\n"
    "v_mfma_f32_16x16x32_bf16 a[148:151], a[204:207], a[12:15], a[148:151]\n"
    "s_cbranch_vccnz .Lpad6_%=\n"
    ".Lret6_%=:\n"
    "s_add_u32 s46, s39, 0x0\n"
    "v_max3_f32 v217, v217, v234, v235\n"
    "v_max3_f32 v217, v217, v236, v237\n"
    "v_mfma_f32_16x16x32_bf16 a[156:159], a[200:203], a[40:43], a[156:159]\n"
    "s_add_u32 m0, s42, 0x18000\n"
    "v_cvt_pk_bf16_f32 v173, v174, v175\n"
    "v_accvgpr_read_b32 v230, a160\n"
    "v_mfma_f32_16x16x32_bf16 a[156:159], a[204:207], a[44:47], a[156:159]\n"
    "buffer_load_dwordx4 %[voe], %[srq], s46 offen nt lds\n"
    "v_mfma_f32_16x16x32_bf16 a[164:167], a[200:203], a[72:75], a[164:167]\n"
    "ds_read_b128 a[196:199], %[kb1] offset:0\n"
    "ds_read_b128 a[200:203], %[kb0] offset:1024\n"
    "v_mfma_f32_16x16x32_bf16 a[164:167], a[204:207], a[76:79], a[164:167]\n"
    "ds_write_b64 %[stw], v[172:173] offset:128\n"
    "v_mfma_f32_16x16x32_bf16 a[148:151], a[208:211], a[16:19], a[148:151]\n"
    "v_accvgpr_read_b32 v231, a161\n"
    "v_accvgpr_read_b32 v232, a162\n"
    "v_mfma_f32_16x16x32_bf16 a[148:151], a[212:215], a[20:23], a[148:151]\n"
    "v_accvgpr_read_b32 v233, a163\n"
    "v_max3_f32 v218, v218, v230, v231\n"
    "v_mfma_f32_16x16x32_bf16 a[156:159], a[208:211], a[48:51], a[156:159]\n"
    "v_max3_f32 v218, v218, v232, v233\n"
    "v_mul_f32 v180, v220, v180\n"
    "v_mfma_f32_16x16x32_bf16 a[156:159], a[212:215], a[52:55], a[156:159]\n"
    "ds_read_b128 a[204:207], %[kb1] offset:1024\n"
    "v_mul_f32 v181, v220, v181\n"
    "v_mfma_f32_16x16x32_bf16 a[164:167], a[208:211], a[80:83], a[164:167]\n"
    "ds_read_b128 a[208:211], %[kb0] offset:2048\n"
    "v_mfma_f32_16x16x32_bf16 a[164:167], a[212:215], a[84:87], a[164:167]\n"
    "s_waitcnt lgkmcnt(5)\n"
    "v_mfma_f32_16x16x32_bf16 a[148:151], a[216:219], a[24:27], a[148:151]\n"
    "s_add_u32 s47, s41, 0x1000\n"
    "v_mul_f32 v182, v220, v182\n"
    "v_mfma_f32_16x16x32_bf16 a[148:151], a[220:223], a[28:31], a[148:151]\n"
    "s_add_u32 m0, s42, 0x18380\n"
    "v_mfma_f32_16x16x32_bf16 a[156:159], a[216:219], a[56:59], a[156:159]\n"
    "v_mul_f32 v183, v220, v183\n"
    "v_mfma_f32_16x16x32_bf16 a[156:159], a[220:223], a[60:63], a[156:159]\n"
    "ds_read_b128 a[212:215], %[kb1] offset:2048\n"
    "v_mfma_f32_16x16x32_bf16 a[164:167], a[216:219], a[88:91], a[164:167]\n"
    "ds_read_b128 a[216:219], %[kb0] offset:3072\n"
    "v_mfma_f32_16x16x32_bf16 a[164:167], a[220:223], a[92:95], a[164:167]\n"
    "buffer_load_dwordx4 %[voe], %[srq], s46 offen offset:128 nt lds\n"
    "v_mfma_f32_16x16x32_bf16 v[48:51], a[192:195], a[0:3], 0\n"
    "s_waitcnt lgkmcnt(4)\n"
    "v_cvt_pk_bf16_f32 v180, v180, v181\n"
    "v_accvgpr_read_b32 v234, a148\n"
    "v_mfma_f32_16x16x32_bf16 v[48:51], a[196:199], a[4:7], v[48:51]\n"
    "s_add_u32 m0, s42, 0x18700\n"
    "v_accvgpr_read_b32 v235, a149\n"
    "v_accvgpr_read_b32 v236, a150\n"
    "v_mfma_f32_16x16x32_bf16 v[56:59], a[192:195], a[32:35], 0\n"
    "v_accvgpr_read_b32 v237, a151\n"
    "v_max3_f32 v216, v216, v234, v235\n"
    "v_mfma_f32_16x16x32_bf16 v[56:59], a[196:199], a[36:39], v[56:59]\n"
    "ds_read_b128 a[220:223], %[kb1] offset:3072\n"
    "v_max3_f32 v216, v216, v236, v237\n"
    "v_mfma_f32_16x16x32_bf16 v[64:67], a[192:195], a[64:67], 0\n"
    "ds_read_b128 a[192:195], %[kb0] offset:8192\n"
    "v_cvt_pk_bf16_f32 v181, v182, v183\n"
    "v_mfma_f32_16x16x32_bf16 v[64:67], a[196:199], a[68:71], v[64:67]\n"
    "buffer_load_dwordx4 %[voe], %[srq], s46 offen offset:256 nt lds\n"
    "v_mfma_f32_16x16x32_bf16 v[48:51], a[200:203], a[8:11], v[48:51]\n"
    "s_waitcnt lgkmcnt(4)\n"
    "v_accvgpr_read_b32 v230, a156\n"
    "v_accvgpr_read_b32 v231, a157\n"
    "v_mfma_f32_16x16x32_bf16 v[48:51], a[204:207], a[12:15], v[48:51]\n"
    "s_add_u32 m0, s42, 0x18a80\n"
    "ds_write_b64 %[stw], v[180:181] offset:160\n"
    "v_mfma_f32_16x16x32_bf16 v[56:59], a[200:203], a[40:43], v[56:59]\n"
    "v_accvgpr_read_b32 v232, a158\n"
    "v_accvgpr_read_b32 v233, a159\n"
    "v_mfma_f32_16x16x32_bf16 v[56:59], a[204:207], a[44:47], v[56:59]\n"
    "ds_read_b128 a[196:199], %[kb1] offset:8192\n"
    "v_max3_f32 v217, v217, v230, v231\n"
    "v_mfma_f32_16x16x32_bf16 v[64:67], a[200:203], a[72:75], v[64:67]\n"
    "ds_read_b128 a[200:203], %[kb0] offset:9216\n"
    "v_max3_f32 v217, v217, v232, v233\n"
    "v_mfma_f32_16x16x32_bf16 v[64:67], a[204:207], a[76:79], v[64:67]\n"
    "buffer_load_dwordx4 %[voe], %[srq], s46 offen offset:384 nt lds\n"
    "v_mfma_f32_16x16x32_bf16 v[48:51], a[208:211], a[16:19], v[48:51]\n"
    "s_waitcnt lgkmcnt(5)\n"
    "buffer_store_dwordx4 a[248:251], %[vos], %[sro], s47 offen offset:256 sc0 sc1\n"
    "v_mfma_f32_16x16x32_bf16 v[48:51], a[212:215], a[20:23], v[48:51]\n"
    "v_accvgpr_read_b32 v234, a164\n"
    "v_accvgpr_read_b32 v235, a165\n"
    "v_mfma_f32_16x16x32_bf16 v[56:59], a[208:211], a[48:51], v[56:59]\n"
    "v_accvgpr_read_b32 v236, a166\n"
    "v_accvgpr_read_b32 v237, a167\n"
    "v_mfma_f32_16x16x32_bf16 v[56:59], a[212:215], a[52:55], v[56:59]\n"
    "s_cbranch_vccnz .Lpad7_%=\n"
    ".Lret7_%=:\n"
    "ds_read_b128 a[204:207], %[kb1] offset:9216\n"
    "v_max3_f32 v218, v218, v234, v235\n"
    "v_mfma_f32_16x16x32_bf16 v[64:67], a[208:211], a[80:83], v[64:67]\n"
    "ds_read_b128 a[208:211], %[kb0] offset:10240\n"
    "v_max3_f32 v218, v218, v236, v237\n"
    "v_mfma_f32_16x16x32_bf16 v[64:67], a[212:215], a[84:87], v[64:67]\n"
    "v_mul_f32 v188, v220, v188\n"
    "v_mul_f32 v189, v220, v189\n"
    "v_mfma_f32_16x16x32_bf16 v[48:51], a[216:219], a[24:27], v[48:51]\n"
    "s_waitcnt lgkmcnt(4)\n"
    "v_mul_f32 v190, v220, v190\n"
    "v_mul_f32 v191, v220, v191\n"
    "v_mfma_f32_16x16x32_bf16 v[48:51], a[220:223], a[28:31], v[48:51]\n"
    "v_mfma_f32_16x16x32_bf16 v[56:59], a[216:219], a[56:59], v[56:59]\n"
    "v_cvt_pk_bf16_f32 v188, v188, v189\n"
    "v_mfma_f32_16x16x32_bf16 v[56:59], a[220:223], a[60:63], v[56:59]\n"
    "ds_read_b128 a[212:215], %[kb1] offset:10240\n"
    "v_mfma_f32_16x16x32_bf16 v[64:67], a[216:219], a[88:91], v[64:67]\n"
    "ds_read_b128 a[216:219], %[kb0] offset:11264\n"
    "v_cvt_pk_bf16_f32 v189, v190, v191\n"
    "v_mfma_f32_16x16x32_bf16 v[64:67], a[220:223], a[92:95], v[64:67]\n"
    "v_mfma_f32_16x16x32_bf16 v[52:55], a[192:195], a[0:3], 0\n"
    "s_waitcnt lgkmcnt(4)\n"
    "v_max3_f32 v216, v216, v48, v49\n"
    "v_max3_f32 v216, v216, v50, v51\n"
    "v_mfma_f32_16x16x32_bf16 v[52:55], a[196:199], a[4:7], v[52:55]\n"
    "v_mfma_f32_16x16x32_bf16 v[60:63], a[192:195], a[32:35], 0\n"
    "v_max3_f32 v217, v217, v56, v57\n"
    "v_max3_f32 v217, v217, v58, v59\n"
    "v_mfma_f32_16x16x32_bf16 v[60:63], a[196:199], a[36:39], v[60:63]\n"
    "ds_read_b128 a[220:223], %[kb1] offset:11264\n"
    "v_mfma_f32_16x16x32_bf16 v[68:71], a[192:195], a[64:67], 0\n"
    "s_waitcnt vmcnt(21)\n"
    "s_barrier\n"
    "ds_read_b128 a[192:195], %[kb0] offset:49152\n"
    "v_max3_f32 v218, v218, v64, v65\n"
    "v_mfma_f32_16x16x32_bf16 v[68:71], a[196:199], a[68:71], v[68:71]\n"
    "ds_write_b64 %[stw], v[188:189] offset:192\n"
    "v_mfma_f32_16x16x32_bf16 v[52:55], a[200:203], a[8:11], v[52:55]\n"
    "s_waitcnt lgkmcnt(4)\n"
    "v_max3_f32 v218, v218, v66, v67\n"
    "v_mul_f32 v196, v220, v196\n"
    "v_mfma_f32_16x16x32_bf16 v[52:55], a[204:207], a[12:15], v[52:55]\n"
    "s_add_u32 s46, s36, 0x2c000\n"
    "s_add_u32 m0, s42, 0x14000\n"
    "v_mfma_f32_16x16x32_bf16 v[60:63], a[200:203], a[40:43], v[60:63]\n"
    "buffer_load_dwordx4 %[vok], %[srk], s46 offen nt lds\n"
    "v_mfma_f32_16x16x32_bf16 v[60:63], a[204:207], a[44:47], v[60:63]\n"
    "ds_read_b128 a[196:199], %[kb1] offset:49152\n"
    "v_mul_f32 v197, v220, v197\n"
    "v_mfma_f32_16x16x32_bf16 v[68:71], a[200:203], a[72:75], v[68:71]\n"
    "s_add_u32 m0, s42, 0x14380\n"
    "ds_read_b128 a[200:203], %[kb0] offset:50176\n"
    "v_mfma_f32_16x16x32_bf16 v[68:71], a[204:207], a[76:79], v[68:71]\n"
    "v_mul_f32 v198, v220, v198\n"
    "v_mfma_f32_16x16x32_bf16 v[52:55], a[208:211], a[16:19], v[52:55]\n"
    "v_mfma_f32_16x16x32_bf16 v[52:55], a[212:215], a[20:23], v[52:55]\n"
    "buffer_load_dwordx4 %[vok], %[srk], s46 offen offset:128 nt lds\n"
    "v_mfma_f32_16x16x32_bf16 v[60:63], a[208:211], a[48:51], v[60:63]\n"
    "v_mul_f32 v199, v220, v199\n"
    "v_mfma_f32_16x16x32_bf16 v[60:63], a[212:215], a[52:55], v[60:63]\n"
    "ds_read_b128 a[204:207], %[kb1] offset:50176\n"
    "v_mfma_f32_16x16x32_bf16 v[68:71], a[208:211], a[80:83], v[68:71]\n"
    "ds_read_b128 a[208:211], %[kb0] offset:51200\n"
    "v_cvt_pk_bf16_f32 v196, v196, v197\n"
    "v_mfma_f32_16x16x32_bf16 v[68:71], a[212:215], a[84:87], v[68:71]\n"
    "s_waitcnt lgkmcnt(4)\n"
    "v_mfma_f32_16x16x32_bf16 v[52:55], a[216:219], a[24:27], v[52:55]\n"
    "v_mfma_f32_16x16x32_bf16 v[52:55], a[220:223], a[28:31], v[52:55]\n"
    "v_cvt_pk_bf16_f32 v197, v198, v199\n"
    "v_mfma_f32_16x16x32_bf16 v[60:63], a[216:219], a[56:59], v[60:63]\n"
    "buffer_store_dwordx4 a[252:255], %[vos], %[sro], s47 offen offset:2304 sc0 sc1\n"
    "v_mfma_f32_16x16x32_bf16 v[60:63], a[220:223], a[60:63], v[60:63]\n"
    "s_add_u32 m0, s42, 0x14700\n"
    "ds_read_b128 a[212:215], %[kb1] offset:51200\n"
    "v_mfma_f32_16x16x32_bf16 v[68:71], a[216:219], a[88:91], v[68:71]\n"
    "ds_read_b128 a[216:219], %[kb0] offset:52224\n"
    "v_mfma_f32_16x16x32_bf16 v[68:71], a[220:223], a[92:95], v[68:71]\n"
    "s_cbranch_vccnz .Lpad8_%=\n"
    ".Lret8_%=:\n"
    "v_mfma_f32_16x16x32_bf16 v[72:75], a[192:195], a[0:3], 0\n"
    "s_waitcnt lgkmcnt(4)\n"
    "buffer_load_dwordx4 %[vok], %[srk], s46 offen offset:256 nt lds\n"
    "v_mfma_f32_16x16x32_bf16 v[72:75], a[196:199], a[4:7], v[72:75]\n"
    "ds_write_b64 %[stw], v[196:197] offset:224\n"
    "v_mfma_f32_16x16x32_bf16 v[80:83], a[192:195], a[32:35], 0\n"
    "v_max3_f32 v216, v216, v52, v53\n"
    "v_max3_f32 v216, v216, v54, v55\n"
    "v_mfma_f32_16x16x32_bf16 v[80:83], a[196:199], a[36:39], v[80:83]\n"
    "s_add_u32 m0, s42, 0x14a80\n"
    "ds_read_b128 a[220:223], %[kb1] offset:52224\n"
    "v_max3_f32 v217, v217, v60, v61\n"
    "v_mfma_f32_16x16x32_bf16 v[88:91], a[192:195], a[64:67], 0\n"
    "ds_read_b128 a[192:195], %[kb0] offset:57344\n"
    "v_max3_f32 v217, v217, v62, v63\n"
    "v_mfma_f32_16x16x32_bf16 v[88:91], a[196:199], a[68:71], v[88:91]\n"
    "ds_read_b128 a[240:243], %[str] offset:0\n"
    "v_max3_f32 v218, v218, v68, v69\n"
    "v_mfma_f32_16x16x32_bf16 v[72:75], a[200:203], a[8:11], v[72:75]\n"
    "s_waitcnt lgkmcnt(6)\n"
    "buffer_load_dwordx4 %[vok], %[srk], s46 offen offset:384 nt lds\n"
    "v_mfma_f32_16x16x32_bf16 v[72:75], a[204:207], a[12:15], v[72:75]\n"
    "ds_read_b128 a[244:247], %[str] offset:1088\n"
    "v_max3_f32 v218, v218, v70, v71\n"
    "v_mfma_f32_16x16x32_bf16 v[80:83], a[200:203], a[40:43], v[80:83]\n"
    "ds_read_b128 a[248:251], %[str] offset:2176\n"
    "v_mfma_f32_16x16x32_bf16 v[80:83], a[204:207], a[44:47], v[80:83]\n"
    "ds_read_b128 a[196:199], %[kb1] offset:57344\n"
    "v_mfma_f32_16x16x32_bf16 v[88:91], a[200:203], a[72:75], v[88:91]\n"
    "ds_read_b128 a[200:203], %[kb0] offset:58368\n"
    "v_mfma_f32_16x16x32_bf16 v[88:91], a[204:207], a[76:79], v[88:91]\n"
    "ds_read_b128 a[252:255], %[str] offset:3264\n"
    "v_mfma_f32_16x16x32_bf16 v[72:75], a[208:211], a[16:19], v[72:75]\n"
    "s_waitcnt lgkmcnt(8)\n"
    "v_mfma_f32_16x16x32_bf16 v[72:75], a[212:215], a[20:23], v[72:75]\n"
    "v_mfma_f32_16x16x32_bf16 v[80:83], a[208:211], a[48:51], v[80:83]\n"
    "v_mfma_f32_16x16x32_bf16 v[80:83], a[212:215], a[52:55], v[80:83]\n"
    "ds_read_b128 a[204:207], %[kb1] offset:58368\n"
    "v_mfma_f32_16x16x32_bf16 v[88:91], a[208:211], a[80:83], v[88:91]\n"
    "ds_read_b128 a[208:211], %[kb0] offset:59392\n"
    "v_mfma_f32_16x16x32_bf16 v[88:91], a[212:215], a[84:87], v[88:91]\n"
    "v_mfma_f32_16x16x32_bf16 v[72:75], a[216:219], a[24:27], v[72:75]\n"
    "s_waitcnt lgkmcnt(5)\n"
    "v_mfma_f32_16x16x32_bf16 v[72:75], a[220:223], a[28:31], v[72:75]\n"
    "v_mfma_f32_16x16x32_bf16 v[80:83], a[216:219], a[56:59], v[80:83]\n"
    "v_mfma_f32_16x16x32_bf16 v[80:83], a[220:223], a[60:63], v[80:83]\n"
    "ds_read_b128 a[212:215], %[kb1] offset:59392\n"
    "v_mfma_f32_16x16x32_bf16 v[88:91], a[216:219], a[88:91], v[88:91]\n"
    "ds_read_b128 a[216:219], %[kb0] offset:60416\n"
    "v_mfma_f32_16x16x32_bf16 v[88:91], a[220:223], a[92:95], v[88:91]\n"
    "v_mfma_f32_16x16x32_bf16 v[76:79], a[192:195], a[0:3], 0\n"
    "s_waitcnt lgkmcnt(4)\n"
    "v_max3_f32 v216, v216, v72, v73\n"
    "v_max3_f32 v216, v216, v74, v75\n"
    "v_mfma_f32_16x16x32_bf16 v[76:79], a[196:199], a[4:7], v[76:79]\n"
    "v_mfma_f32_16x16x32_bf16 v[84:87], a[192:195], a[32:35], 0\n"
    "v_max3_f32 v217, v217, v80, v81\n"
    "v_max3_f32 v217, v217, v82, v83\n"
    "v_mfma_f32_16x16x32_bf16 v[84:87], a[196:199], a[36:39], v[84:87]\n"
    "ds_read_b128 a[220:223], %[kb1] offset:60416\n"
    "v_mfma_f32_16x16x32_bf16 v[92:95], a[192:195], a[64:67], 0\n"
    "s_waitcnt vmcnt(21)\n"
    "s_barrier\n"
    "v_add_u32 %[kb0], 0xffff0000, %[kb0]\n"
    "v_add_u32 %[kb1], 0xffff0000, %[kb1]\n"
    "v_mfma_f32_16x16x32_bf16 v[92:95], a[196:199], a[68:71], v[92:95]\n"
    "ds_read_b128 a[192:195], %[kb0] offset:16384\n"
    "v_max3_f32 v218, v218, v88, v89\n"
    "v_mfma_f32_16x16x32_bf16 v[76:79], a[200:203], a[8:11], v[76:79]\n"
    "s_waitcnt lgkmcnt(3)\n"
    "v_max3_f32 v218, v218, v90, v91\n"
    "s_add_u32 s46, s38, 0x0\n"
    "v_mfma_f32_16x16x32_bf16 v[76:79], a[204:207], a[12:15], v[76:79]\n"
    "s_cbranch_vccnz .Lpad9_%=\n"
    ".Lret9_%=:\n"
    "s_add_u32 m0, s42, 0x10000\n"
    "v_mfma_f32_16x16x32_bf16 v[84:87], a[200:203], a[40:43], v[84:87]\n"
    "buffer_load_dwordx4 %[vov], %[srv], s46 offen sc0 sc1 lds\n"
    "v_mfma_f32_16x16x32_bf16 v[84:87], a[204:207], a[44:47], v[84:87]\n"
    "ds_read_b128 a[196:199], %[kb1] offset:16384\n"
    "v_mfma_f32_16x16x32_bf16 v[92:95], a[200:203], a[72:75], v[92:95]\n"
    "ds_read_b128 a[200:203], %[kb0] offset:17408\n"
    "v_mul_f32 v204, v221, v204\n"
    "v_mfma_f32_16x16x32_bf16 v[92:95], a[204:207], a[76:79], v[92:95]\n"
    "s_add_u32 m0, s42, 0x10380\n"
    "v_mul_f32 v205, v221, v205\n"
    "v_mul_f32 v206, v221, v206\n"
    "v_mfma_f32_16x16x32_bf16 v[76:79], a[208:211], a[16:19], v[76:79]\n"
    "v_mul_f32 v207, v221, v207\n"
    "v_cvt_pk_bf16_f32 v204, v204, v205\n"
    "v_mfma_f32_16x16x32_bf16 v[76:79], a[212:215], a[20:23], v[76:79]\n"
    "v_cvt_pk_bf16_f32 v205, v206, v207\n"
    "v_mfma_f32_16x16x32_bf16 v[84:87], a[208:211], a[48:51], v[84:87]\n"
    "buffer_load_dwordx4 %[vov], %[srv], s46 offen offset:128 sc0 sc1 lds\n"
    "v_mfma_f32_16x16x32_bf16 v[84:87], a[212:215], a[52:55], v[84:87]\n"
    "ds_read_b128 a[204:207], %[kb1] offset:17408\n"
    "v_mfma_f32_16x16x32_bf16 v[92:95], a[208:211], a[80:83], v[92:95]\n"
    "s_add_u32 s47, s41, 0x2000\n"
    "ds_read_b128 a[208:211], %[kb0] offset:18432\n"
    "v_mfma_f32_16x16x32_bf16 v[92:95], a[212:215], a[84:87], v[92:95]\n"
    "s_waitcnt lgkmcnt(4)\n"
    "ds_write_b64 %[stw], v[204:205] offset:0\n"
    "v_mfma_f32_16x16x32_bf16 v[76:79], a[216:219], a[24:27], v[76:79]\n"
    "s_add_u32 s46, s38, 0x1000\n"
    "v_mul_f32 v212, v221, v212\n"
    "v_mul_f32 v213, v221, v213\n"
    "v_mfma_f32_16x16x32_bf16 v[76:79], a[220:223], a[28:31], v[76:79]\n"
    "s_add_u32 m0, s42, 0x10800\n"
    "v_mul_f32 v214, v221, v214\n"
    "v_mul_f32 v215, v221, v215\n"
    "v_mfma_f32_16x16x32_bf16 v[84:87], a[216:219], a[56:59], v[84:87]\n"
    "buffer_load_dwordx4 %[vov], %[srv], s46 offen sc0 sc1 lds\n"
    "v_mfma_f32_16x16x32_bf16 v[84:87], a[220:223], a[60:63], v[84:87]\n"
    "ds_read_b128 a[212:215], %[kb1] offset:18432\n"
    "v_cvt_pk_bf16_f32 v212, v212, v213\n"
    "v_mfma_f32_16x16x32_bf16 v[92:95], a[216:219], a[88:91], v[92:95]\n"
    "ds_read_b128 a[216:219], %[kb0] offset:19456\n"
    "v_cvt_pk_bf16_f32 v213, v214, v215\n"
    "v_mfma_f32_16x16x32_bf16 v[92:95], a[220:223], a[92:95], v[92:95]\n"
    "s_add_u32 m0, s42, 0x10b80\n"
    "ds_write_b64 %[stw], v[212:213] offset:32\n"
    "v_mfma_f32_16x16x32_bf16 v[96:99], a[192:195], a[0:3], 0\n"
    "s_waitcnt lgkmcnt(6)\n"
    "v_accvgpr_read_b32 v222, a168\n"
    "v_accvgpr_read_b32 v223, a169\n"
    "v_mfma_f32_16x16x32_bf16 v[96:99], a[196:199], a[4:7], v[96:99]\n"
    "v_accvgpr_read_b32 v224, a170\n"
    "v_accvgpr_read_b32 v225, a171\n"
    "v_mfma_f32_16x16x32_bf16 v[104:107], a[192:195], a[32:35], 0\n"
    "buffer_load_dwordx4 %[vov], %[srv], s46 offen offset:128 sc0 sc1 lds\n"
    "v_mfma_f32_16x16x32_bf16 v[104:107], a[196:199], a[36:39], v[104:107]\n"
    "ds_read_b128 a[220:223], %[kb1] offset:19456\n"
    "v_max3_f32 v216, v216, v76, v77\n"
    "v_mfma_f32_16x16x32_bf16 v[112:115], a[192:195], a[64:67], 0\n"
    "ds_read_b128 a[192:195], %[kb0] offset:24576\n"
    "v_max3_f32 v216, v216, v78, v79\n"
    "v_mfma_f32_16x16x32_bf16 v[112:115], a[196:199], a[68:71], v[112:115]\n"
    "v_mul_f32 v222, v221, v222\n"
    "v_max3_f32 v217, v217, v84, v85\n"
    "v_mfma_f32_16x16x32_bf16 v[96:99], a[200:203], a[8:11], v[96:99]\n"
    "s_waitcnt lgkmcnt(5)\n"
    "v_max3_f32 v217, v217, v86, v87\n"
    "v_mul_f32 v223, v221, v223\n"
    "v_mfma_f32_16x16x32_bf16 v[96:99], a[204:207], a[12:15], v[96:99]\n"
    "v_max3_f32 v218, v218, v92, v93\n"
    "v_max3_f32 v218, v218, v94, v95\n"
    "v_mfma_f32_16x16x32_bf16 v[104:107], a[200:203], a[40:43], v[104:107]\n"
    "v_mul_f32 v224, v221, v224\n"
    "v_mul_f32 v225, v221, v225\n"
    "v_mfma_f32_16x16x32_bf16 v[104:107], a[204:207], a[44:47], v[104:107]\n"
    "ds_read_b128 a[196:199], %[kb1] offset:24576\n"
    "v_mfma_f32_16x16x32_bf16 v[112:115], a[200:203], a[72:75], v[112:115]\n"
    "ds_read_b128 a[200:203], %[kb0] offset:25600\n"
    "v_cvt_pk_bf16_f32 v222, v222, v223\n"
    "v_mfma_f32_16x16x32_bf16 v[112:115], a[204:207], a[76:79], v[112:115]\n"
    "buffer_store_dwordx4 a[240:243], %[vos], %[sro], s47 offen offset:256 sc0 sc1\n"
    "v_mfma_f32_16x16x32_bf16 v[96:99], a[208:211], a[16:19], v[96:99]\n"
    "s_waitcnt lgkmcnt(4)\n"
    "v_cvt_pk_bf16_f32 v223, v224, v225\n"
    "v_mfma_f32_16x16x32_bf16 v[96:99], a[212:215], a[20:23], v[96:99]\n"
    "v_mfma_f32_16x16x32_bf16 v[104:107], a[208:211], a[48:51], v[104:107]\n"
    "v_mfma_f32_16x16x32_bf16 v[104:107], a[212:215], a[52:55], v[104:107]\n"
    "s_cbranch_vccnz .Lpad10_%=\n"
    ".Lret10_%=:\n"
    "ds_read_b128 a[204:207], %[kb1] offset:25600\n"
    "v_mfma_f32_16x16x32_bf16 v[112:115], a[208:211], a[80:83], v[112:115]\n"
    "ds_read_b128 a[208:211], %[kb0] offset:26624\n"
    "v_mfma_f32_16x16x32_bf16 v[112:115], a[212:215], a[84:87], v[112:115]\n"
    "ds_write_b64 %[stw], v[222:223] offset:64\n"
    "v_mfma_f32_16x16x32_bf16 v[96:99], a[216:219], a[24:27], v[96:99]\n"
    "s_waitcnt lgkmcnt(5)\n"
    "v_accvgpr_read_b32 v226, a172\n"
    "v_mfma_f32_16x16x32_bf16 v[96:99], a[220:223], a[28:31], v[96:99]\n"
    "v_mfma_f32_16x16x32_bf16 v[104:107], a[216:219], a[56:59], v[104:107]\n"
    "v_accvgpr_read_b32 v227, a173\n"
    "v_mfma_f32_16x16x32_bf16 v[104:107], a[220:223], a[60:63], v[104:107]\n"
    "ds_read_b128 a[212:215], %[kb1] offset:26624\n"
    "v_mfma_f32_16x16x32_bf16 v[112:115], a[216:219], a[88:91], v[112:115]\n"
    "ds_read_b128 a[216:219], %[kb0] offset:27648\n"
    "v_accvgpr_read_b32 v228, a174\n"
    "v_mfma_f32_16x16x32_bf16 v[112:115], a[220:223], a[92:95], v[112:115]\n"
    "v_mfma_f32_16x16x32_bf16 v[100:103], a[192:195], a[0:3], 0\n"
    "s_waitcnt lgkmcnt(5)\n"
    "v_accvgpr_read_b32 v229, a175\n"
    "v_max3_f32 v216, v216, v96, v97\n"
    "v_mfma_f32_16x16x32_bf16 v[100:103], a[196:199], a[4:7], v[100:103]\n"
    "v_max3_f32 v216, v216, v98, v99\n"
    "v_mfma_f32_16x16x32_bf16 v[108:111], a[192:195], a[32:35], 0\n"
    "v_mul_f32 v226, v221, v226\n"
    "v_max3_f32 v217, v217, v104, v105\n"
    "v_mfma_f32_16x16x32_bf16 v[108:111], a[196:199], a[36:39], v[108:111]\n"
    "ds_read_b128 a[220:223], %[kb1] offset:27648\n"
    "v_max3_f32 v217, v217, v106, v107\n"
    "v_mfma_f32_16x16x32_bf16 v[116:119], a[192:195], a[64:67], 0\n"
    "s_waitcnt vmcnt(21)\n"
    "s_barrier\n"
    "ds_read_b128 a[192:195], %[kb0] offset:49152\n"
    "v_mul_f32 v227, v221, v227\n"
    "v_mfma_f32_16x16x32_bf16 v[116:119], a[196:199], a[68:71], v[116:119]\n"
    "v_mul_f32 v228, v221, v228\n"
    "v_max3_f32 v218, v218, v112, v113\n"
    "v_mfma_f32_16x16x32_bf16 v[100:103], a[200:203], a[8:11], v[100:103]\n"
    "s_waitcnt lgkmcnt(3)\n"
    "v_max3_f32 v218, v218, v114, v115\n"
    "s_add_u32 s46, s39, 0x1000\n"
    "v_mfma_f32_16x16x32_bf16 v[100:103], a[204:207], a[12:15], v[100:103]\n"
    "s_add_u32 m0, s42, 0x1c000\n"
    "v_mul_f32 v229, v221, v229\n"
    "v_mfma_f32_16x16x32_bf16 v[108:111], a[200:203], a[40:43], v[108:111]\n"
    "buffer_load_dwordx4 %[voo], %[srq], s46 offen nt lds\n"
    "v_mfma_f32_16x16x32_bf16 v[108:111], a[204:207], a[44:47], v[108:111]\n"
    "ds_read_b128 a[196:199], %[kb1] offset:49152\n"
    "v_cvt_pk_bf16_f32 v226, v226, v227\n"
    "v_mfma_f32_16x16x32_bf16 v[116:119], a[200:203], a[72:75], v[116:119]\n"
    "ds_read_b128 a[200:203], %[kb0] offset:50176\n"
    "v_mfma_f32_16x16x32_bf16 v[116:119], a[204:207], a[76:79], v[116:119]\n"
    "v_cvt_pk_bf16_f32 v227, v228, v229\n"
    "v_mfma_f32_16x16x32_bf16 v[100:103], a[208:211], a[16:19], v[100:103]\n"
    "v_mfma_f32_16x16x32_bf16 v[100:103], a[212:215], a[20:23], v[100:103]\n"
    "v_mfma_f32_16x16x32_bf16 v[108:111], a[208:211], a[48:51], v[108:111]\n"
    "v_mfma_f32_16x16x32_bf16 v[108:111], a[212:215], a[52:55], v[108:111]\n"
    "ds_read_b128 a[204:207], %[kb1] offset:50176\n"
    "v_mfma_f32_16x16x32_bf16 v[116:119], a[208:211], a[80:83], v[116:119]\n"
    "ds_read_b128 a[208:211], %[kb0] offset:51200\n"
    "v_mfma_f32_16x16x32_bf16 v[116:119], a[212:215], a[84:87], v[116:119]\n"
    "s_waitcnt lgkmcnt(4)\n"
    "ds_write_b64 %[stw], v[226:227] offset:96\n"
    "v_mfma_f32_16x16x32_bf16 v[100:103], a[216:219], a[24:27], v[100:103]\n"
    "buffer_store_dwordx4 a[244:247], %[vos], %[sro], s47 offen offset:2304 sc0 sc1\n"
    "v_mfma_f32_16x16x32_bf16 v[100:103], a[220:223], a[28:31], v[100:103]\n"
    "s_add_u32 m0, s42, 0x1c380\n"
    "v_accvgpr_read_b32 v222, a176\n"
    "v_accvgpr_read_b32 v223, a177\n"
    "v_mfma_f32_16x16x32_bf16 v[108:111], a[216:219], a[56:59], v[108:111]\n"
    "v_mfma_f32_16x16x32_bf16 v[108:111], a[220:223], a[60:63], v[108:111]\n"
    "ds_read_b128 a[212:215], %[kb1] offset:51200\n"
    "v_accvgpr_read_b32 v224, a178\n"
    "v_mfma_f32_16x16x32_bf16 v[116:119], a[216:219], a[88:91], v[116:119]\n"
    "buffer_load_dwordx4 %[voo], %[srq], s46 offen offset:128 nt lds\n"
    "v_mfma_f32_16x16x32_bf16 v[116:119], a[220:223], a[92:95], v[116:119]\n"
    "s_cbranch_vccnz .Lpad11_%=\n"
    ".Lret11_%=:\n"
    "ds_read_b128 a[216:219], %[kb0] offset:52224\n"
    "v_accvgpr_read_b32 v225, a179\n"
    "v_mfma_f32_16x16x32_bf16 v[120:123], a[192:195], a[0:3], 0\n"
    "s_waitcnt lgkmcnt(5)\n"
    "v_max3_f32 v216, v216, v100, v101\n"
    "v_max3_f32 v216, v216, v102, v103\n"
    "v_mfma_f32_16x16x32_bf16 v[120:123], a[196:199], a[4:7], v[120:123]\n"
    "s_add_u32 m0, s42, 0x1c700\n"
    "v_mul_f32 v222, v221, v222\n"
    "v_mfma_f32_16x16x32_bf16 v[128:131], a[192:195], a[32:35], 0\n"
    "v_mul_f32 v223, v221, v223\n"
    "v_max3_f32 v217, v217, v108, v109\n"
    "v_mfma_f32_16x16x32_bf16 v[128:131], a[196:199], a[36:39], v[128:131]\n"
    "ds_read_b128 a[220:223], %[kb1] offset:52224\n"
    "v_max3_f32 v217, v217, v110, v111\n"
    "v_mfma_f32_16x16x32_bf16 v[136:139], a[192:195], a[64:67], 0\n"
    "buffer_load_dwordx4 %[voo], %[srq], s46 offen offset:256 nt lds\n"
    "v_mfma_f32_16x16x32_bf16 v[136:139], a[196:199], a[68:71], v[136:139]\n"
    "ds_read_b128 a[192:195], %[kb0] offset:57344\n"
    "v_mul_f32 v224, v221, v224\n"
    "v_mfma_f32_16x16x32_bf16 v[120:123], a[200:203], a[8:11], v[120:123]\n"
    "s_waitcnt lgkmcnt(4)\n"
    "v_max3_f32 v218, v218, v116, v117\n"
    "v_max3_f32 v218, v218, v118, v119\n"
    "v_mfma_f32_16x16x32_bf16 v[120:123], a[204:207], a[12:15], v[120:123]\n"
    "s_add_u32 m0, s42, 0x1ca80\n"
    "v_mul_f32 v225, v221, v225\n"
    "v_mfma_f32_16x16x32_bf16 v[128:131], a[200:203], a[40:43], v[128:131]\n"
    "v_cvt_pk_bf16_f32 v222, v222, v223\n"
    "v_mfma_f32_16x16x32_bf16 v[128:131], a[204:207], a[44:47], v[128:131]\n"
    "ds_read_b128 a[196:199], %[kb1] offset:57344\n"
    "v_mfma_f32_16x16x32_bf16 v[136:139], a[200:203], a[72:75], v[136:139]\n"
    "buffer_load_dwordx4 %[voo], %[srq], s46 offen offset:384 nt lds\n"
    "v_mfma_f32_16x16x32_bf16 v[136:139], a[204:207], a[76:79], v[136:139]\n"
    "ds_read_b128 a[200:203], %[kb0] offset:58368\n"
    "v_cvt_pk_bf16_f32 v223, v224, v225\n"
    "v_mfma_f32_16x16x32_bf16 v[120:123], a[208:211], a[16:19], v[120:123]\n"
    "s_waitcnt lgkmcnt(4)\n"
    "v_mfma_f32_16x16x32_bf16 v[120:123], a[212:215], a[20:23], v[120:123]\n"
    "v_mfma_f32_16x16x32_bf16 v[128:131], a[208:211], a[48:51], v[128:131]\n"
    "ds_write_b64 %[stw], v[222:223] offset:128\n"
    "v_mfma_f32_16x16x32_bf16 v[128:131], a[212:215], a[52:55], v[128:131]\n"
    "ds_read_b128 a[204:207], %[kb1] offset:58368\n"
    "v_mfma_f32_16x16x32_bf16 v[136:139], a[208:211], a[80:83], v[136:139]\n"
    "ds_read_b128 a[208:211], %[kb0] offset:59392\n"
    "v_accvgpr_read_b32 v226, a180\n"
    "v_mfma_f32_16x16x32_bf16 v[136:139], a[212:215], a[84:87], v[136:139]\n"
    "v_mfma_f32_16x16x32_bf16 v[120:123], a[216:219], a[24:27], v[120:123]\n"
    "s_waitcnt lgkmcnt(5)\n"
    "v_accvgpr_read_b32 v227, a181\n"
    "v_mfma_f32_16x16x32_bf16 v[120:123], a[220:223], a[28:31], v[120:123]\n"
    "v_mfma_f32_16x16x32_bf16 v[128:131], a[216:219], a[56:59], v[128:131]\n"
    "v_accvgpr_read_b32 v228, a182\n"
    "v_mfma_f32_16x16x32_bf16 v[128:131], a[220:223], a[60:63], v[128:131]\n"
    "ds_read_b128 a[212:215], %[kb1] offset:59392\n"
    "v_mfma_f32_16x16x32_bf16 v[136:139], a[216:219], a[88:91], v[136:139]\n"
    "ds_read_b128 a[216:219], %[kb0] offset:60416\n"
    "v_accvgpr_read_b32 v229, a183\n"
    "v_mfma_f32_16x16x32_bf16 v[136:139], a[220:223], a[92:95], v[136:139]\n"
    "v_mfma_f32_16x16x32_bf16 v[124:127], a[192:195], a[0:3], 0\n"
    "s_waitcnt lgkmcnt(4)\n"
    "v_mul_f32 v226, v221, v226\n"
    "v_max3_f32 v216, v216, v120, v121\n"
    "v_mfma_f32_16x16x32_bf16 v[124:127], a[196:199], a[4:7], v[124:127]\n"
    "v_mul_f32 v227, v221, v227\n"
    "v_max3_f32 v216, v216, v122, v123\n"
    "v_mfma_f32_16x16x32_bf16 v[132:135], a[192:195], a[32:35], 0\n"
    "s_add_u32 s47, s41, 0x3000\n"
    "v_max3_f32 v217, v217, v128, v129\n"
    "v_max3_f32 v217, v217, v130, v131\n"
    "v_mfma_f32_16x16x32_bf16 v[132:135], a[196:199], a[36:39], v[132:135]\n"
    "ds_read_b128 a[220:223], %[kb1] offset:60416\n"
    "v_mul_f32 v228, v221, v228\n"
    "v_mfma_f32_16x16x32_bf16 v[140:143], a[192:195], a[64:67], 0\n"
    "s_waitcnt vmcnt(21)\n"
    "s_barrier\n"
    "ds_read_b128 a[192:195], %[kb0] offset:32768\n"
    "v_max3_f32 v218, v218, v136, v137\n"
    "v_mfma_f32_16x16x32_bf16 v[140:143], a[196:199], a[68:71], v[140:143]\n"
    "v_mul_f32 v229, v221, v229\n"
    "v_max3_f32 v218, v218, v138, v139\n"
    "v_mfma_f32_16x16x32_bf16 v[124:127], a[200:203], a[8:11], v[124:127]\n"
    "s_waitcnt lgkmcnt(3)\n"
    "s_add_u32 s46, s38, 0x8000\n"
    "s_add_u32 m0, s42, 0x4000\n"
    "v_mfma_f32_16x16x32_bf16 v[124:127], a[204:207], a[12:15], v[124:127]\n"
    "s_cbranch_vccnz .Lpad12_%=\n"
    ".Lret12_%=:\n"
    "buffer_load_dwordx4 %[vov], %[srv], s46 offen sc0 sc1 lds\n"
    "v_mfma_f32_16x16x32_bf16 v[132:135], a[200:203], a[40:43], v[132:135]\n"
    "v_cvt_pk_bf16_f32 v226, v226, v227\n"
    "v_mfma_f32_16x16x32_bf16 v[132:135], a[204:207], a[44:47], v[132:135]\n"
    "ds_read_b128 a[196:199], %[kb1] offset:32768\n"
    "v_cvt_pk_bf16_f32 v227, v228, v229\n"
    "v_mfma_f32_16x16x32_bf16 v[140:143], a[200:203], a[72:75], v[140:143]\n"
    "s_add_u32 m0, s42, 0x4380\n"
    "ds_read_b128 a[200:203], %[kb0] offset:33792\n"
    "v_mfma_f32_16x16x32_bf16 v[140:143], a[204:207], a[76:79], v[140:143]\n"
    "v_mfma_f32_16x16x32_bf16 v[124:127], a[208:211], a[16:19], v[124:127]\n"
    "v_mfma_f32_16x16x32_bf16 v[124:127], a[212:215], a[20:23], v[124:127]\n"
    "buffer_load_dwordx4 %[vov], %[srv], s46 offen offset:128 sc0 sc1 lds\n"
    "v_mfma_f32_16x16x32_bf16 v[132:135], a[208:211], a[48:51], v[132:135]\n"
    "ds_write_b64 %[stw], v[226:227] offset:160\n"
    "v_mfma_f32_16x16x32_bf16 v[132:135], a[212:215], a[52:55], v[132:135]\n"
    "ds_read_b128 a[204:207], %[kb1] offset:33792\n"
    "v_accvgpr_read_b32 v222, a184\n"
    "v_mfma_f32_16x16x32_bf16 v[140:143], a[208:211], a[80:83], v[140:143]\n"
    "s_add_u32 s46, s38, 0x9000\n"
    "ds_read_b128 a[208:211], %[kb0] offset:34816\n"
    "s_add_u32 m0, s42, 0x4800\n"
    "v_mfma_f32_16x16x32_bf16 v[140:143], a[212:215], a[84:87], v[140:143]\n"
    "s_waitcnt lgkmcnt(5)\n"
    "v_accvgpr_read_b32 v223, a185\n"
    "v_mfma_f32_16x16x32_bf16 v[124:127], a[216:219], a[24:27], v[124:127]\n"
    "v_mfma_f32_16x16x32_bf16 v[124:127], a[220:223], a[28:31], v[124:127]\n"
    "buffer_load_dwordx4 %[vov], %[srv], s46 offen sc0 sc1 lds\n"
    "v_mfma_f32_16x16x32_bf16 v[132:135], a[216:219], a[56:59], v[132:135]\n"
    "buffer_store_dwordx4 a[248:251], %[vos], %[sro], s47 offen offset:256 sc0 sc1\n"
    "v_mfma_f32_16x16x32_bf16 v[132:135], a[220:223], a[60:63], v[132:135]\n"
    "ds_read_b128 a[212:215], %[kb1] offset:34816\n"
    "v_accvgpr_read_b32 v224, a186\n"
    "v_mfma_f32_16x16x32_bf16 v[140:143], a[216:219], a[88:91], v[140:143]\n"
    "s_add_u32 m0, s42, 0x4b80\n"
    "ds_read_b128 a[216:219], %[kb0] offset:35840\n"
    "v_accvgpr_read_b32 v225, a187\n"
    "v_mfma_f32_16x16x32_bf16 v[140:143], a[220:223], a[92:95], v[140:143]\n"
    "v_mul_f32 v222, v221, v222\n"
    "v_mfma_f32_16x16x32_bf16 v[144:147], a[192:195], a[0:3], 0\n"
    "s_waitcnt lgkmcnt(4)\n"
    "v_mul_f32 v223, v221, v223\n"
    "v_max3_f32 v216, v216, v124, v125\n"
    "v_mfma_f32_16x16x32_bf16 v[144:147], a[196:199], a[4:7], v[144:147]\n"
    "buffer_load_dwordx4 %[vov], %[srv], s46 offen offset:128 sc0 sc1 lds\n"
    "v_mfma_f32_16x16x32_bf16 v[152:155], a[192:195], a[32:35], 0\n"
    "v_max3_f32 v216, v216, v126, v127\n"
    "v_mul_f32 v224, v221, v224\n"
    "v_mfma_f32_16x16x32_bf16 v[152:155], a[196:199], a[36:39], v[152:155]\n"
    "ds_read_b128 a[220:223], %[kb1] offset:35840\n"
    "v_max3_f32 v217, v217, v132, v133\n"
    "v_mfma_f32_16x16x32_bf16 v[160:163], a[192:195], a[64:67], 0\n"
    "ds_read_b128 a[192:195], %[kb0] offset:40960\n"
    "v_max3_f32 v217, v217, v134, v135\n"
    "v_mfma_f32_16x16x32_bf16 v[160:163], a[196:199], a[68:71], v[160:163]\n"
    "v_mul_f32 v225, v221, v225\n"
    "v_max3_f32 v218, v218, v140, v141\n"
    "v_mfma_f32_16x16x32_bf16 v[144:147], a[200:203], a[8:11], v[144:147]\n"
    "s_waitcnt lgkmcnt(4)\n"
    "v_max3_f32 v218, v218, v142, v143\n"
    "v_cvt_pk_bf16_f32 v222, v222, v223\n"
    "v_mfma_f32_16x16x32_bf16 v[144:147], a[204:207], a[12:15], v[144:147]\n"
    "v_mfma_f32_16x16x32_bf16 v[152:155], a[200:203], a[40:43], v[152:155]\n"
    "v_cvt_pk_bf16_f32 v223, v224, v225\n"
    "v_mfma_f32_16x16x32_bf16 v[152:155], a[204:207], a[44:47], v[152:155]\n"
    "ds_read_b128 a[196:199], %[kb1] offset:40960\n"
    "v_mfma_f32_16x16x32_bf16 v[160:163], a[200:203], a[72:75], v[160:163]\n"
    "ds_read_b128 a[200:203], %[kb0] offset:41984\n"
    "v_mfma_f32_16x16x32_bf16 v[160:163], a[204:207], a[76:79], v[160:163]\n"
    "v_mfma_f32_16x16x32_bf16 v[144:147], a[208:211], a[16:19], v[144:147]\n"
    "s_waitcnt lgkmcnt(4)\n"
    "ds_write_b64 %[stw], v[222:223] offset:192\n"
    "v_mfma_f32_16x16x32_bf16 v[144:147], a[212:215], a[20:23], v[144:147]\n"
    "v_mfma_f32_16x16x32_bf16 v[152:155], a[208:211], a[48:51], v[152:155]\n"
    "v_accvgpr_read_b32 v226, a188\n"
    "v_mfma_f32_16x16x32_bf16 v[152:155], a[212:215], a[52:55], v[152:155]\n"
    "s_cbranch_vccnz .Lpad13_%=\n"
    ".Lret13_%=:\n"
    "ds_read_b128 a[204:207], %[kb1] offset:41984\n"
    "v_mfma_f32_16x16x32_bf16 v[160:163], a[208:211], a[80:83], v[160:163]\n"
    "ds_read_b128 a[208:211], %[kb0] offset:43008\n"
    "v_accvgpr_read_b32 v227, a189\n"
    "v_mfma_f32_16x16x32_bf16 v[160:163], a[212:215], a[84:87], v[160:163]\n"
    "v_mfma_f32_16x16x32_bf16 v[144:147], a[216:219], a[24:27], v[144:147]\n"
    "s_waitcnt lgkmcnt(5)\n"
    "v_accvgpr_read_b32 v228, a190\n"
    "v_mfma_f32_16x16x32_bf16 v[144:147], a[220:223], a[28:31], v[144:147]\n"
    "v_mfma_f32_16x16x32_bf16 v[152:155], a[216:219], a[56:59], v[152:155]\n"
    "v_accvgpr_read_b32 v229, a191\n"
    "v_mfma_f32_16x16x32_bf16 v[152:155], a[220:223], a[60:63], v[152:155]\n"
    "ds_read_b128 a[212:215], %[kb1] offset:43008\n"
    "v_mul_f32 v226, v221, v226\n"
    "v_mfma_f32_16x16x32_bf16 v[160:163], a[216:219], a[88:91], v[160:163]\n"
    "ds_read_b128 a[216:219], %[kb0] offset:44032\n"
    "v_mfma_f32_16x16x32_bf16 v[160:163], a[220:223], a[92:95], v[160:163]\n"
    "v_mul_f32 v227, v221, v227\n"
    "v_mfma_f32_16x16x32_bf16 v[148:151], a[192:195], a[0:3], 0\n"
    "s_waitcnt lgkmcnt(4)\n"
    "v_max3_f32 v216, v216, v144, v145\n"
    "v_max3_f32 v216, v216, v146, v147\n"
    "v_mfma_f32_16x16x32_bf16 v[148:151], a[196:199], a[4:7], v[148:151]\n"
    "v_mul_f32 v228, v221, v228\n"
    "v_mfma_f32_16x16x32_bf16 v[156:159], a[192:195], a[32:35], 0\n"
    "v_max3_f32 v217, v217, v152, v153\n"
    "v_max3_f32 v217, v217, v154, v155\n"
    "v_mfma_f32_16x16x32_bf16 v[156:159], a[196:199], a[36:39], v[156:159]\n"
    "ds_read_b128 a[220:223], %[kb1] offset:44032\n"
    "v_mul_f32 v229, v221, v229\n"
    "v_mfma_f32_16x16x32_bf16 v[164:167], a[192:195], a[64:67], 0\n"
    "s_waitcnt vmcnt(26)\n"
    "s_barrier\n"
    "ds_read_b128 a[192:195], %[kb0] offset:0\n"
    "v_max3_f32 v218, v218, v160, v161\n"
    "v_mfma_f32_16x16x32_bf16 v[164:167], a[196:199], a[68:71], v[164:167]\n"
    "buffer_store_dwordx4 a[252:255], %[vos], %[sro], s47 offen offset:2304 sc0 sc1\n"
    "v_mfma_f32_16x16x32_bf16 v[148:151], a[200:203], a[8:11], v[148:151]\n"
    "s_waitcnt lgkmcnt(3)\n"
    "v_cvt_pk_bf16_f32 v226, v226, v227\n"
    "v_max3_f32 v218, v218, v162, v163\n"
    "v_mfma_f32_16x16x32_bf16 v[148:151], a[204:207], a[12:15], v[148:151]\n"
    "s_add_u32 s46, s38, 0x10000\n"
    "v_cvt_pk_bf16_f32 v227, v228, v229\n"
    "s_add_u32 m0, s42, 0xc000\n"
    "v_mfma_f32_16x16x32_bf16 v[156:159], a[200:203], a[40:43], v[156:159]\n"
    "buffer_load_dwordx4 %[vov], %[srv], s46 offen sc0 sc1 lds\n"
    "v_mfma_f32_16x16x32_bf16 v[156:159], a[204:207], a[44:47], v[156:159]\n"
    "ds_read_b128 a[196:199], %[kb1] offset:0\n"
    "v_mfma_f32_16x16x32_bf16 v[164:167], a[200:203], a[72:75], v[164:167]\n"
    "s_add_u32 m0, s42, 0xc380\n"
    "ds_read_b128 a[200:203], %[kb0] offset:1024\n"
    "v_mfma_f32_16x16x32_bf16 v[164:167], a[204:207], a[76:79], v[164:167]\n"
    "ds_write_b64 %[stw], v[226:227] offset:224\n"
    "v_mfma_f32_16x16x32_bf16 v[148:151], a[208:211], a[16:19], v[148:151]\n"
    "v_mfma_f32_16x16x32_bf16 v[148:151], a[212:215], a[20:23], v[148:151]\n"
    "buffer_load_dwordx4 %[vov], %[srv], s46 offen offset:128 sc0 sc1 lds\n"
    "v_mfma_f32_16x16x32_bf16 v[156:159], a[208:211], a[48:51], v[156:159]\n"
    "ds_read_b128 a[240:243], %[str] offset:0\n"
    "v_mfma_f32_16x16x32_bf16 v[156:159], a[212:215], a[52:55], v[156:159]\n"
    "ds_read_b128 a[204:207], %[kb1] offset:1024\n"
    "v_mfma_f32_16x16x32_bf16 v[164:167], a[208:211], a[80:83], v[164:167]\n"
    "s_add_u32 s46, s38, 0x11000\n"
    "ds_read_b128 a[208:211], %[kb0] offset:2048\n"
    "s_add_u32 m0, s42, 0xc800\n"
    "v_mfma_f32_16x16x32_bf16 v[164:167], a[212:215], a[84:87], v[164:167]\n"
    "s_waitcnt lgkmcnt(6)\n"
    "ds_read_b128 a[244:247], %[str] offset:1088\n"
    "v_mfma_f32_16x16x32_bf16 v[148:151], a[216:219], a[24:27], v[148:151]\n"
    "ds_read_b128 a[248:251], %[str] offset:2176\n"
    "v_mfma_f32_16x16x32_bf16 v[148:151], a[220:223], a[28:31], v[148:151]\n"
    "buffer_load_dwordx4 %[vov], %[srv], s46 offen sc0 sc1 lds\n"
    "v_mfma_f32_16x16x32_bf16 v[156:159], a[216:219], a[56:59], v[156:159]\n"
    "ds_read_b128 a[252:255], %[str] offset:3264\n"
    "v_mfma_f32_16x16x32_bf16 v[156:159], a[220:223], a[60:63], v[156:159]\n"
    "ds_read_b128 a[212:215], %[kb1] offset:2048\n"
    "v_mfma_f32_16x16x32_bf16 v[164:167], a[216:219], a[88:91], v[164:167]\n"
    "s_add_u32 m0, s42, 0xcb80\n"
    "ds_read_b128 a[216:219], %[kb0] offset:3072\n"
    "v_mfma_f32_16x16x32_bf16 v[164:167], a[220:223], a[92:95], v[164:167]\n"
    "s_cbranch_vccnz .Lpad14_%=\n"
    ".Lret14_%=:\n"
    "v_mfma_f32_16x16x32_bf16 v[168:171], a[192:195], a[0:3], 0\n"
    "s_waitcnt lgkmcnt(7)\n"
    "v_max3_f32 v216, v216, v148, v149\n"
    "v_max3_f32 v216, v216, v150, v151\n"
    "v_mfma_f32_16x16x32_bf16 v[168:171], a[196:199], a[4:7], v[168:171]\n"
    "buffer_load_dwordx4 %[vov], %[srv], s46 offen offset:128 sc0 sc1 lds\n"
    "v_mfma_f32_16x16x32_bf16 v[176:179], a[192:195], a[32:35], 0\n"
    "v_max3_f32 v217, v217, v156, v157\n"
    "v_max3_f32 v217, v217, v158, v159\n"
    "v_mfma_f32_16x16x32_bf16 v[176:179], a[196:199], a[36:39], v[176:179]\n"
    "ds_read_b128 a[220:223], %[kb1] offset:3072\n"
    "v_mfma_f32_16x16x32_bf16 v[184:187], a[192:195], a[64:67], 0\n"
    "ds_read_b128 a[192:195], %[kb0] offset:8192\n"
    "v_max3_f32 v218, v218, v164, v165\n"
    "v_mfma_f32_16x16x32_bf16 v[184:187], a[196:199], a[68:71], v[184:187]\n"
    "v_max3_f32 v218, v218, v166, v167\n"
    "v_mfma_f32_16x16x32_bf16 v[168:171], a[200:203], a[8:11], v[168:171]\n"
    "s_waitcnt lgkmcnt(4)\n"
    "v_mfma_f32_16x16x32_bf16 v[168:171], a[204:207], a[12:15], v[168:171]\n"
    "v_mfma_f32_16x16x32_bf16 v[176:179], a[200:203], a[40:43], v[176:179]\n"
    "v_mfma_f32_16x16x32_bf16 v[176:179], a[204:207], a[44:47], v[176:179]\n"
    "ds_read_b128 a[196:199], %[kb1] offset:8192\n"
    "v_mfma_f32_16x16x32_bf16 v[184:187], a[200:203], a[72:75], v[184:187]\n"
    "ds_read_b128 a[200:203], %[kb0] offset:9216\n"
    "v_mfma_f32_16x16x32_bf16 v[184:187], a[204:207], a[76:79], v[184:187]\n"
    "v_mfma_f32_16x16x32_bf16 v[168:171], a[208:211], a[16:19], v[168:171]\n"
    "s_waitcnt lgkmcnt(4)\n"
    "v_mfma_f32_16x16x32_bf16 v[168:171], a[212:215], a[20:23], v[168:171]\n"
    "v_mfma_f32_16x16x32_bf16 v[176:179], a[208:211], a[48:51], v[176:179]\n"
    "v_mfma_f32_16x16x32_bf16 v[176:179], a[212:215], a[52:55], v[176:179]\n"
    "ds_read_b128 a[204:207], %[kb1] offset:9216\n"
    "v_mfma_f32_16x16x32_bf16 v[184:187], a[208:211], a[80:83], v[184:187]\n"
    "ds_read_b128 a[208:211], %[kb0] offset:10240\n"
    "v_mfma_f32_16x16x32_bf16 v[184:187], a[212:215], a[84:87], v[184:187]\n"
    "v_mfma_f32_16x16x32_bf16 v[168:171], a[216:219], a[24:27], v[168:171]\n"
    "s_waitcnt lgkmcnt(4)\n"
    "v_mfma_f32_16x16x32_bf16 v[168:171], a[220:223], a[28:31], v[168:171]\n"
    "v_mfma_f32_16x16x32_bf16 v[176:179], a[216:219], a[56:59], v[176:179]\n"
    "v_mfma_f32_16x16x32_bf16 v[176:179], a[220:223], a[60:63], v[176:179]\n"
    "ds_read_b128 a[212:215], %[kb1] offset:10240\n"
    "v_mfma_f32_16x16x32_bf16 v[184:187], a[216:219], a[88:91], v[184:187]\n"
    "ds_read_b128 a[216:219], %[kb0] offset:11264\n"
    "v_mfma_f32_16x16x32_bf16 v[184:187], a[220:223], a[92:95], v[184:187]\n"
    "v_mfma_f32_16x16x32_bf16 v[172:175], a[192:195], a[0:3], 0\n"
    "s_waitcnt lgkmcnt(4)\n"
    "v_max3_f32 v216, v216, v168, v169\n"
    "v_max3_f32 v216, v216, v170, v171\n"
    "v_mfma_f32_16x16x32_bf16 v[172:175], a[196:199], a[4:7], v[172:175]\n"
    "s_add_u32 s47, s41, 0x4000\n"
    "v_mfma_f32_16x16x32_bf16 v[180:183], a[192:195], a[32:35], 0\n"
    "v_max3_f32 v217, v217, v176, v177\n"
    "v_max3_f32 v217, v217, v178, v179\n"
    "v_mfma_f32_16x16x32_bf16 v[180:183], a[196:199], a[36:39], v[180:183]\n"
    "ds_read_b128 a[220:223], %[kb1] offset:11264\n"
    "v_mfma_f32_16x16x32_bf16 v[188:191], a[192:195], a[64:67], 0\n"
    "s_waitcnt vmcnt(20)\n"
    "s_barrier\n"
    "v_add_u32 %[kb0], 0x10000, %[kb0]\n"
    "v_add_u32 %[kb1], 0x10000, %[kb1]\n"
    "v_mfma_f32_16x16x32_bf16 v[188:191], a[196:199], a[68:71], v[188:191]\n"
    "ds_read_b128 a[192:195], %[kb0] offset:16384\n"
    "v_max3_f32 v218, v218, v184, v185\n"
    "v_mfma_f32_16x16x32_bf16 v[172:175], a[200:203], a[8:11], v[172:175]\n"
    "s_waitcnt lgkmcnt(3)\n"
    "v_max3_f32 v218, v218, v186, v187\n"
    "s_add_u32 s46, s38, 0x18000\n"
    "v_mfma_f32_16x16x32_bf16 v[172:175], a[204:207], a[12:15], v[172:175]\n"
    "s_cbranch_vccnz .Lpad15_%=\n"
    ".Lret15_%=:\n"
    "s_add_u32 m0, s42, 0x8000\n"
    "v_mfma_f32_16x16x32_bf16 v[180:183], a[200:203], a[40:43], v[180:183]\n"
    "buffer_load_dwordx4 %[vov], %[srv], s46 offen sc0 sc1 lds\n"
    "v_mfma_f32_16x16x32_bf16 v[180:183], a[204:207], a[44:47], v[180:183]\n"
    "ds_read_b128 a[196:199], %[kb1] offset:16384\n"
    "v_mfma_f32_16x16x32_bf16 v[188:191], a[200:203], a[72:75], v[188:191]\n"
    "s_add_u32 m0, s42, 0x8380\n"
    "ds_read_b128 a[200:203], %[kb0] offset:17408\n"
    "v_mfma_f32_16x16x32_bf16 v[188:191], a[204:207], a[76:79], v[188:191]\n"
    "buffer_store_dwordx4 a[240:243], %[vos], %[sro], s47 offen offset:256 sc0 sc1\n"
    "v_mfma_f32_16x16x32_bf16 v[172:175], a[208:211], a[16:19], v[172:175]\n"
    "v_mfma_f32_16x16x32_bf16 v[172:175], a[212:215], a[20:23], v[172:175]\n"
    "buffer_load_dwordx4 %[vov], %[srv], s46 offen offset:128 sc0 sc1 lds\n"
    "v_mfma_f32_16x16x32_bf16 v[180:183], a[208:211], a[48:51], v[180:183]\n"
    "v_mfma_f32_16x16x32_bf16 v[180:183], a[212:215], a[52:55], v[180:183]\n"
    "ds_read_b128 a[204:207], %[kb1] offset:17408\n"
    "v_mfma_f32_16x16x32_bf16 v[188:191], a[208:211], a[80:83], v[188:191]\n"
    "ds_read_b128 a[208:211], %[kb0] offset:18432\n"
    "v_mfma_f32_16x16x32_bf16 v[188:191], a[212:215], a[84:87], v[188:191]\n"
    "s_waitcnt lgkmcnt(4)\n"
    "v_mfma_f32_16x16x32_bf16 v[172:175], a[216:219], a[24:27], v[172:175]\n"
    "v_mfma_f32_16x16x32_bf16 v[172:175], a[220:223], a[28:31], v[172:175]\n"
    "v_mfma_f32_16x16x32_bf16 v[180:183], a[216:219], a[56:59], v[180:183]\n"
    "v_mfma_f32_16x16x32_bf16 v[180:183], a[220:223], a[60:63], v[180:183]\n"
    "ds_read_b128 a[212:215], %[kb1] offset:18432\n"
    "v_mfma_f32_16x16x32_bf16 v[188:191], a[216:219], a[88:91], v[188:191]\n"
    "s_add_u32 s46, s38, 0x19000\n"
    "ds_read_b128 a[216:219], %[kb0] offset:19456\n"
    "s_add_u32 m0, s42, 0x8800\n"
    "v_mfma_f32_16x16x32_bf16 v[188:191], a[220:223], a[92:95], v[188:191]\n"
    "buffer_store_dwordx4 a[244:247], %[vos], %[sro], s47 offen offset:2304 sc0 sc1\n"
    "v_mfma_f32_16x16x32_bf16 v[192:195], a[192:195], a[0:3], 0\n"
    "s_waitcnt lgkmcnt(4)\n"
    "v_max3_f32 v216, v216, v172, v173\n"
    "v_max3_f32 v216, v216, v174, v175\n"
    "v_mfma_f32_16x16x32_bf16 v[192:195], a[196:199], a[4:7], v[192:195]\n"
    "buffer_load_dwordx4 %[vov], %[srv], s46 offen sc0 sc1 lds\n"
    "v_mfma_f32_16x16x32_bf16 v[200:203], a[192:195], a[32:35], 0\n"
    "v_max3_f32 v217, v217, v180, v181\n"
    "v_max3_f32 v217, v217, v182, v183\n"
    "v_mfma_f32_16x16x32_bf16 v[200:203], a[196:199], a[36:39], v[200:203]\n"
    "ds_read_b128 a[220:223], %[kb1] offset:19456\n"
    "v_mfma_f32_16x16x32_bf16 v[208:211], a[192:195], a[64:67], 0\n"
    "ds_read_b128 a[192:195], %[kb0] offset:24576\n"
    "v_max3_f32 v218, v218, v188, v189\n"
    "v_mfma_f32_16x16x32_bf16 v[208:211], a[196:199], a[68:71], v[208:211]\n"
    "v_max3_f32 v218, v218, v190, v191\n"
    "v_mfma_f32_16x16x32_bf16 v[192:195], a[200:203], a[8:11], v[192:195]\n"
    "s_waitcnt lgkmcnt(4)\n"
    "v_mfma_f32_16x16x32_bf16 v[192:195], a[204:207], a[12:15], v[192:195]\n"
    "v_mfma_f32_16x16x32_bf16 v[200:203], a[200:203], a[40:43], v[200:203]\n"
    "v_mfma_f32_16x16x32_bf16 v[200:203], a[204:207], a[44:47], v[200:203]\n"
    "ds_read_b128 a[196:199], %[kb1] offset:24576\n"
    "v_mfma_f32_16x16x32_bf16 v[208:211], a[200:203], a[72:75], v[208:211]\n"
    "ds_read_b128 a[200:203], %[kb0] offset:25600\n"
    "v_mfma_f32_16x16x32_bf16 v[208:211], a[204:207], a[76:79], v[208:211]\n"
    "s_add_u32 s47, s41, 0x5000\n"
    "v_mfma_f32_16x16x32_bf16 v[192:195], a[208:211], a[16:19], v[192:195]\n"
    "s_waitcnt lgkmcnt(4)\n"
    "s_add_u32 m0, s42, 0x8b80\n"
    "v_mfma_f32_16x16x32_bf16 v[192:195], a[212:215], a[20:23], v[192:195]\n"
    "v_mfma_f32_16x16x32_bf16 v[200:203], a[208:211], a[48:51], v[200:203]\n"
    "v_mfma_f32_16x16x32_bf16 v[200:203], a[212:215], a[52:55], v[200:203]\n"
    "s_cbranch_vccnz .Lpad16_%=\n"
    ".Lret16_%=:\n"
    "buffer_load_dwordx4 %[vov], %[srv], s46 offen offset:128 sc0 sc1 lds\n"
    "v_mfma_f32_16x16x32_bf16 v[208:211], a[208:211], a[80:83], v[208:211]\n"
    "ds_read_b128 a[204:207], %[kb1] offset:25600\n"
    "ds_read_b128 a[208:211], %[kb0] offset:26624\n"
    "v_mfma_f32_16x16x32_bf16 v[208:211], a[212:215], a[84:87], v[208:211]\n"
    "v_mfma_f32_16x16x32_bf16 v[192:195], a[216:219], a[24:27], v[192:195]\n"
    "s_waitcnt lgkmcnt(4)\n"
    "buffer_store_dwordx4 a[248:251], %[vos], %[sro], s47 offen offset:256 sc0 sc1\n"
    "v_mfma_f32_16x16x32_bf16 v[192:195], a[220:223], a[28:31], v[192:195]\n"
    "v_mfma_f32_16x16x32_bf16 v[200:203], a[216:219], a[56:59], v[200:203]\n"
    "v_mfma_f32_16x16x32_bf16 v[200:203], a[220:223], a[60:63], v[200:203]\n"
    "ds_read_b128 a[212:215], %[kb1] offset:26624\n"
    "v_mfma_f32_16x16x32_bf16 v[208:211], a[216:219], a[88:91], v[208:211]\n"
    "ds_read_b128 a[216:219], %[kb0] offset:27648\n"
    "v_mfma_f32_16x16x32_bf16 v[208:211], a[220:223], a[92:95], v[208:211]\n"
    "v_mfma_f32_16x16x32_bf16 v[196:199], a[192:195], a[0:3], 0\n"
    "s_waitcnt lgkmcnt(4)\n"
    "v_max3_f32 v216, v216, v192, v193\n"
    "v_max3_f32 v216, v216, v194, v195\n"
    "v_mfma_f32_16x16x32_bf16 v[196:199], a[196:199], a[4:7], v[196:199]\n"
    "v_mfma_f32_16x16x32_bf16 v[204:207], a[192:195], a[32:35], 0\n"
    "v_max3_f32 v217, v217, v200, v201\n"
    "v_max3_f32 v217, v217, v202, v203\n"
    "v_mfma_f32_16x16x32_bf16 v[204:207], a[196:199], a[36:39], v[204:207]\n"
    "ds_read_b128 a[220:223], %[kb1] offset:27648\n"
    "v_mfma_f32_16x16x32_bf16 v[212:215], a[192:195], a[64:67], 0\n"
    "s_waitcnt vmcnt(23)\n"
    "s_barrier\n"
    "ds_read_b64_tr_b16 a[192:193], %[vb0] offset:0\n"
    "ds_read_b64_tr_b16 a[194:195], %[vb0] offset:4096\n"
    "v_mfma_f32_16x16x32_bf16 v[212:215], a[196:199], a[68:71], v[212:215]\n"
    "v_max3_f32 v218, v218, v208, v209\n"
    "v_max3_f32 v218, v218, v210, v211\n"
    "v_mfma_f32_16x16x32_bf16 v[196:199], a[200:203], a[8:11], v[196:199]\n"
    "s_waitcnt lgkmcnt(4)\n"
    "v_mfma_f32_16x16x32_bf16 v[196:199], a[204:207], a[12:15], v[196:199]\n"
    "v_mfma_f32_16x16x32_bf16 v[204:207], a[200:203], a[40:43], v[204:207]\n"
    "buffer_store_dwordx4 a[252:255], %[vos], %[sro], s47 offen offset:2304 sc0 sc1\n"
    "v_mfma_f32_16x16x32_bf16 v[204:207], a[204:207], a[44:47], v[204:207]\n"
    "s_add_u32 s46, s38, 0x20000\n"
    "ds_read_b64_tr_b16 a[196:197], %[vb1] offset:0\n"
    "ds_read_b64_tr_b16 a[198:199], %[vb1] offset:4096\n"
    "v_mfma_f32_16x16x32_bf16 v[212:215], a[200:203], a[72:75], v[212:215]\n"
    "s_add_u32 m0, s42, 0x0\n"
    "ds_read_b64_tr_b16 a[200:201], %[vb2] offset:0\n"
    "ds_read_b64_tr_b16 a[202:203], %[vb2] offset:4096\n"
    "v_mfma_f32_16x16x32_bf16 v[212:215], a[204:207], a[76:79], v[212:215]\n"
    "v_mfma_f32_16x16x32_bf16 v[196:199], a[208:211], a[16:19], v[196:199]\n"
    "buffer_load_dwordx4 %[vov], %[srv], s46 offen sc0 sc1 lds\n"
    "v_mfma_f32_16x16x32_bf16 v[196:199], a[212:215], a[20:23], v[196:199]\n"
    "v_mfma_f32_16x16x32_bf16 v[204:207], a[208:211], a[48:51], v[204:207]\n"
    "v_mfma_f32_16x16x32_bf16 v[204:207], a[212:215], a[52:55], v[204:207]\n"
    "ds_read_b64_tr_b16 a[204:205], %[vb3] offset:0\n"
    "ds_read_b64_tr_b16 a[206:207], %[vb3] offset:4096\n"
    "v_mfma_f32_16x16x32_bf16 v[212:215], a[208:211], a[80:83], v[212:215]\n"
    "ds_read_b64_tr_b16 a[208:209], %[vb0] offset:1024\n"
    "ds_read_b64_tr_b16 a[210:211], %[vb0] offset:5120\n"
    "v_mfma_f32_16x16x32_bf16 v[212:215], a[212:215], a[84:87], v[212:215]\n"
    "s_waitcnt lgkmcnt(8)\n"
    "v_mfma_f32_16x16x32_bf16 v[196:199], a[216:219], a[24:27], v[196:199]\n"
    "v_mfma_f32_16x16x32_bf16 v[196:199], a[220:223], a[28:31], v[196:199]\n"
    "v_mfma_f32_16x16x32_bf16 v[204:207], a[216:219], a[56:59], v[204:207]\n"
    "v_mfma_f32_16x16x32_bf16 v[204:207], a[220:223], a[60:63], v[204:207]\n"
    "ds_read_b64_tr_b16 a[212:213], %[vb1] offset:1024\n"
    "ds_read_b64_tr_b16 a[214:215], %[vb1] offset:5120\n"
    "v_mfma_f32_16x16x32_bf16 v[212:215], a[216:219], a[88:91], v[212:215]\n"
    "s_add_u32 m0, s42, 0x380\n"
    "v_mfma_f32_16x16x32_bf16 v[212:215], a[220:223], a[92:95], v[212:215]\n"
    "s_cbranch_vccnz .Lpad17_%=\n"
    ".Lret17_%=:\n"
    "s_nop 0\n"
    "v_max3_f32 v216, v216, v196, v197\n"
    "v_max3_f32 v216, v216, v198, v199\n"
    "v_max3_f32 v217, v217, v204, v205\n"
    "v_max3_f32 v217, v217, v206, v207\n"
    "s_nop 2\n"
    "v_max3_f32 v218, v218, v212, v213\n"
    "v_max3_f32 v218, v218, v214, v215\n"
    "v_mov_b32 v230, v216\n"
    "v_mov_b32 v231, v217\n"
    "v_mov_b32 v232, v218\n"
    "v_permlane16_swap_b32 v216, v230\n"
    "v_permlane16_swap_b32 v217, v231\n"
    "v_permlane16_swap_b32 v218, v232\n"
    "v_max_f32 v216, v216, v230\n"
    "v_max_f32 v217, v217, v231\n"
    "v_max_f32 v218, v218, v232\n"
    "v_mov_b32 v230, v216\n"
    "v_mov_b32 v231, v217\n"
    "v_mov_b32 v232, v218\n"
    "v_permlane32_swap_b32 v216, v230\n"
    "v_permlane32_swap_b32 v217, v231\n"
    "v_permlane32_swap_b32 v218, v232\n"
    "v_max_f32 v216, v216, v230\n"
    "v_max_f32 v217, v217, v231\n"
    "v_max_f32 v218, v218, v232\n"
    "v_mul_f32 v216, s44, v216\n"
    "v_mul_f32 v217, s44, v217\n"
    "v_mul_f32 v218, s44, v218\n"
    "v_pk_fma_f32 v[0:1], v[0:1], s[42:43], v[216:217] op_sel:[0,1,0] op_sel_hi:[1,1,0]\n"
    "v_pk_fma_f32 v[2:3], v[2:3], s[42:43], v[216:217] op_sel:[0,1,0] op_sel_hi:[1,1,0]\n"
    "v_pk_fma_f32 v[4:5], v[4:5], s[42:43], v[216:217] op_sel:[0,1,0] op_sel_hi:[1,1,0]\n"
    "v_pk_fma_f32 v[6:7], v[6:7], s[42:43], v[216:217] op_sel:[0,1,0] op_sel_hi:[1,1,0]\n"
    "v_exp_f32 v0, v0\n"
    "v_exp_f32 v1, v1\n"
    "v_exp_f32 v2, v2\n"
    "v_exp_f32 v3, v3\n"
    "v_exp_f32 v4, v4\n"
    "v_exp_f32 v5, v5\n"
    "v_exp_f32 v6, v6\n"
    "v_exp_f32 v7, v7\n"
    "v_cvt_pk_bf16_f32 v0, v0, v1\n"
    "v_cvt_pk_bf16_f32 v1, v2, v3\n"
    "v_cvt_pk_bf16_f32 v2, v4, v5\n"
    "v_cvt_pk_bf16_f32 v3, v6, v7\n"
    "v_pk_fma_f32 v[8:9], v[8:9], s[42:43], v[216:217] op_sel:[0,1,1] op_sel_hi:[1,1,1]\n"
    "v_pk_fma_f32 v[10:11], v[10:11], s[42:43], v[216:217] op_sel:[0,1,1] op_sel_hi:[1,1,1]\n"
    "v_pk_fma_f32 v[12:13], v[12:13], s[42:43], v[216:217] op_sel:[0,1,1] op_sel_hi:[1,1,1]\n"
    "v_pk_fma_f32 v[14:15], v[14:15], s[42:43], v[216:217] op_sel:[0,1,1] op_sel_hi:[1,1,1]\n"
    "v_exp_f32 v8, v8\n"
    "v_exp_f32 v9, v9\n"
    "v_exp_f32 v10, v10\n"
    "v_exp_f32 v11, v11\n"
    "v_exp_f32 v12, v12\n"
    "v_exp_f32 v13, v13\n"
    "v_exp_f32 v14, v14\n"
    "v_exp_f32 v15, v15\n"
    "v_cvt_pk_bf16_f32 v8, v8, v9\n"
    "v_cvt_pk_bf16_f32 v9, v10, v11\n"
    "v_cvt_pk_bf16_f32 v10, v12, v13\n"
    "v_cvt_pk_bf16_f32 v11, v14, v15\n"
    "v_pk_fma_f32 v[16:17], v[16:17], s[42:43], v[218:219] op_sel:[0,1,0] op_sel_hi:[1,1,0]\n"
    "v_pk_fma_f32 v[18:19], v[18:19], s[42:43], v[218:219] op_sel:[0,1,0] op_sel_hi:[1,1,0]\n"
    "v_pk_fma_f32 v[20:21], v[20:21], s[42:43], v[218:219] op_sel:[0,1,0] op_sel_hi:[1,1,0]\n"
    "v_pk_fma_f32 v[22:23], v[22:23], s[42:43], v[218:219] op_sel:[0,1,0] op_sel_hi:[1,1,0]\n"
    "v_exp_f32 v16, v16\n"
    "v_exp_f32 v17, v17\n"
    "v_exp_f32 v18, v18\n"
    "v_exp_f32 v19, v19\n"
    "v_exp_f32 v20, v20\n"
    "v_exp_f32 v21, v21\n"
    "v_exp_f32 v22, v22\n"
    "v_exp_f32 v23, v23\n"
    "v_cvt_pk_bf16_f32 v16, v16, v17\n"
    "v_cvt_pk_bf16_f32 v17, v18, v19\n"
    "v_cvt_pk_bf16_f32 v18, v20, v21\n"
    "v_cvt_pk_bf16_f32 v19, v22, v23\n"
    "v_mfma_f32_16x16x32_bf16 a[0:3], a[192:195], v[0:3], 0\n"
    "ds_read_b64_tr_b16 a[216:217], %[vb2] offset:1024\n"
    "ds_read_b64_tr_b16 a[218:219], %[vb2] offset:5120\n"
    "v_accvgpr_read_b32 v230, a96\n"
    "v_accvgpr_read_b32 v231, a97\n"
    "v_accvgpr_read_b32 v232, a98\n"
    "v_accvgpr_read_b32 v233, a99\n"
    "v_mfma_f32_16x16x32_bf16 a[32:35], a[192:195], v[8:11], 0\n"
    "buffer_load_dwordx4 %[vov], %[srv], s46 offen offset:128 sc0 sc1 lds\n"
    "v_accvgpr_read_b32 v234, a100\n"
    "v_accvgpr_read_b32 v235, a101\n"
    "v_accvgpr_read_b32 v236, a102\n"
    "v_accvgpr_read_b32 v237, a103\n"
    "v_mfma_f32_16x16x32_bf16 a[64:67], a[192:195], v[16:19], 0\n"
    "s_waitcnt lgkmcnt(2)\n"
    "v_fma_f32 v230, v230, s43, v216\n"
    "v_fma_f32 v231, v231, s43, v216\n"
    "v_fma_f32 v232, v232, s43, v216\n"
    "v_fma_f32 v233, v233, s43, v216\n"
    "v_mfma_f32_16x16x32_bf16 a[4:7], a[196:199], v[0:3], 0\n"
    "ds_read_b64_tr_b16 a[220:221], %[vb3] offset:1024\n"
    "ds_read_b64_tr_b16 a[222:223], %[vb3] offset:5120\n"
    "v_fma_f32 v234, v234, s43, v216\n"
    "v_fma_f32 v235, v235, s43, v216\n"
    "v_fma_f32 v236, v236, s43, v216\n"
    "v_fma_f32 v237, v237, s43, v216\n"
    "v_mfma_f32_16x16x32_bf16 a[36:39], a[196:199], v[8:11], 0\n"
    "s_add_u32 s46, s38, 0x21000\n"
    "v_exp_f32 v230, v230\n"
    "v_exp_f32 v231, v231\n"
    "v_mfma_f32_16x16x32_bf16 a[68:71], a[196:199], v[16:19], 0\n"
    "s_add_u32 m0, s42, 0x800\n"
    "v_exp_f32 v232, v232\n"
    "v_exp_f32 v233, v233\n"
    "v_mfma_f32_16x16x32_bf16 a[8:11], a[200:203], v[0:3], 0\n"
    "ds_read_b64_tr_b16 a[192:193], %[vb0] offset:8192\n"
    "ds_read_b64_tr_b16 a[194:195], %[vb0] offset:12288\n"
    "v_exp_f32 v234, v234\n"
    "v_exp_f32 v235, v235\n"
    "v_mfma_f32_16x16x32_bf16 a[40:43], a[200:203], v[8:11], 0\n"
    "buffer_load_dwordx4 %[vov], %[srv], s46 offen sc0 sc1 lds\n"
    "v_exp_f32 v236, v236\n"
    "v_exp_f32 v237, v237\n"
    "v_cvt_pk_bf16_f32 v4, v230, v231\n"
    "v_mfma_f32_16x16x32_bf16 a[72:75], a[200:203], v[16:19], 0\n"
    "v_cvt_pk_bf16_f32 v5, v232, v233\n"
    "v_cvt_pk_bf16_f32 v6, v234, v235\n"
    "v_cvt_pk_bf16_f32 v7, v236, v237\n"
    "v_accvgpr_read_b32 v230, a104\n"
    "v_mfma_f32_16x16x32_bf16 a[12:15], a[204:207], v[0:3], 0\n"
    "ds_read_b64_tr_b16 a[196:197], %[vb1] offset:8192\n"
    "ds_read_b64_tr_b16 a[198:199], %[vb1] offset:12288\n"
    "v_accvgpr_read_b32 v231, a105\n"
    "v_accvgpr_read_b32 v232, a106\n"
    "v_accvgpr_read_b32 v233, a107\n"
    "v_accvgpr_read_b32 v234, a108\n"
    "v_mfma_f32_16x16x32_bf16 a[44:47], a[204:207], v[8:11], 0\n"
    "s_add_u32 m0, s42, 0xb80\n"
    "v_accvgpr_read_b32 v235, a109\n"
    "v_accvgpr_read_b32 v236, a110\n"
    "v_accvgpr_read_b32 v237, a111\n"
    "v_fma_f32 v230, v230, s43, v217\n"
    "v_mfma_f32_16x16x32_bf16 a[76:79], a[204:207], v[16:19], 0\n"
    "v_fma_f32 v231, v231, s43, v217\n"
    "v_fma_f32 v232, v232, s43, v217\n"
    "v_fma_f32 v233, v233, s43, v217\n"
    "v_fma_f32 v234, v234, s43, v217\n"
    "v_mfma_f32_16x16x32_bf16 a[16:19], a[208:211], v[0:3], 0\n"
    "ds_read_b64_tr_b16 a[200:201], %[vb2] offset:8192\n"
    "ds_read_b64_tr_b16 a[202:203], %[vb2] offset:12288\n"
    "v_fma_f32 v235, v235, s43, v217\n"
    "v_fma_f32 v236, v236, s43, v217\n"
    "v_fma_f32 v237, v237, s43, v217\n"
    "v_exp_f32 v230, v230\n"
    "v_mfma_f32_16x16x32_bf16 a[48:51], a[208:211], v[8:11], 0\n"
    "buffer_load_dwordx4 %[vov], %[srv], s46 offen offset:128 sc0 sc1 lds\n"
    "v_exp_f32 v231, v231\n"
    "v_exp_f32 v232, v232\n"
    "v_mfma_f32_16x16x32_bf16 a[80:83], a[208:211], v[16:19], 0\n"
    "v_exp_f32 v233, v233\n"
    "v_exp_f32 v234, v234\n"
    "v_mfma_f32_16x16x32_bf16 a[20:23], a[212:215], v[0:3], 0\n"
    "ds_read_b64_tr_b16 a[204:205], %[vb3] offset:8192\n"
    "ds_read_b64_tr_b16 a[206:207], %[vb3] offset:12288\n"
    "v_exp_f32 v235, v235\n"
    "v_exp_f32 v236, v236\n"
    "v_mfma_f32_16x16x32_bf16 a[52:55], a[212:215], v[8:11], 0\n"
    "v_exp_f32 v237, v237\n"
    "v_cvt_pk_bf16_f32 v12, v230, v231\n"
    "v_cvt_pk_bf16_f32 v13, v232, v233\n"
    "v_mfma_f32_16x16x32_bf16 a[84:87], a[212:215], v[16:19], 0\n"
    "s_waitcnt lgkmcnt(4)\n"
    "v_cvt_pk_bf16_f32 v14, v234, v235\n"
    "v_cvt_pk_bf16_f32 v15, v236, v237\n"
    "v_accvgpr_read_b32 v230, a112\n"
    "v_accvgpr_read_b32 v231, a113\n"
    "v_mfma_f32_16x16x32_bf16 a[24:27], a[216:219], v[0:3], 0\n"
    "ds_read_b64_tr_b16 a[208:209], %[vb0] offset:9216\n"
    "ds_read_b64_tr_b16 a[210:211], %[vb0] offset:13312\n"
    "v_accvgpr_read_b32 v232, a114\n"
    "v_accvgpr_read_b32 v233, a115\n"
    "v_accvgpr_read_b32 v234, a116\n"
    "v_accvgpr_read_b32 v235, a117\n"
    "v_mfma_f32_16x16x32_bf16 a[56:59], a[216:219], v[8:11], 0\n"
    "v_accvgpr_read_b32 v236, a118\n"
    "v_accvgpr_read_b32 v237, a119\n"
    "v_fma_f32 v230, v230, s43, v218\n"
    "v_fma_f32 v231, v231, s43, v218\n"
    "v_fma_f32 v232, v232, s43, v218\n"
    "v_mfma_f32_16x16x32_bf16 a[88:91], a[216:219], v[16:19], 0\n"
    "v_fma_f32 v233, v233, s43, v218\n"
    "v_fma_f32 v234, v234, s43, v218\n"
    "v_fma_f32 v235, v235, s43, v218\n"
    "v_fma_f32 v236, v236, s43, v218\n"
    "v_mfma_f32_16x16x32_bf16 a[28:31], a[220:223], v[0:3], 0\n"
    "v_fma_f32 v237, v237, s43, v218\n"
    "v_exp_f32 v230, v230\n"
    "v_mfma_f32_16x16x32_bf16 a[60:63], a[220:223], v[8:11], 0\n"
    "v_exp_f32 v231, v231\n"
    "v_exp_f32 v232, v232\n"
    "v_mfma_f32_16x16x32_bf16 a[92:95], a[220:223], v[16:19], 0\n"
    "v_exp_f32 v233, v233\n"
    "v_exp_f32 v234, v234\n"
    "v_mfma_f32_16x16x32_bf16 a[228:231], a[224:227], v[0:3], 0\n"
    "ds_read_b64_tr_b16 a[212:213], %[vb1] offset:9216\n"
    "ds_read_b64_tr_b16 a[214:215], %[vb1] offset:13312\n"
    "v_exp_f32 v235, v235\n"
    "v_exp_f32 v236, v236\n"
    "v_mfma_f32_16x16x32_bf16 a[232:235], a[224:227], v[8:11], 0\n"
    "v_exp_f32 v237, v237\n"
    "v_cvt_pk_bf16_f32 v20, v230, v231\n"
    "v_cvt_pk_bf16_f32 v21, v232, v233\n"
    "v_cvt_pk_bf16_f32 v22, v234, v235\n"
    "v_cvt_pk_bf16_f32 v23, v236, v237\n"
    "v_mfma_f32_16x16x32_bf16 a[236:239], a[224:227], v[16:19], 0\n"
    "v_fma_f32 v24, v24, s43, v216\n"
    "v_fma_f32 v25, v25, s43, v216\n"
    "v_fma_f32 v26, v26, s43, v216\n"
    "v_fma_f32 v27, v27, s43, v216\n"
    "v_fma_f32 v28, v28, s43, v216\n"
    "v_fma_f32 v29, v29, s43, v216\n"
    "v_fma_f32 v30, v30, s43, v216\n"
    "v_mfma_f32_16x16x32_bf16 a[0:3], a[192:195], v[4:7], a[0:3]\n"
    "ds_read_b64_tr_b16 a[216:217], %[vb2] offset:9216\n"
    "ds_read_b64_tr_b16 a[218:219], %[vb2] offset:13312\n"
    "v_fma_f32 v31, v31, s43, v216\n"
    "v_exp_f32 v24, v24\n"
    "v_exp_f32 v25, v25\n"
    "v_exp_f32 v26, v26\n"
    "v_mfma_f32_16x16x32_bf16 a[32:35], a[192:195], v[12:15], a[32:35]\n"
    "v_exp_f32 v27, v27\n"
    "v_exp_f32 v28, v28\n"
    "v_exp_f32 v29, v29\n"
    "v_mfma_f32_16x16x32_bf16 a[64:67], a[192:195], v[20:23], a[64:67]\n"
    "v_exp_f32 v30, v30\n"
    "v_exp_f32 v31, v31\n"
    "v_cvt_pk_bf16_f32 v24, v24, v25\n"
    "v_cvt_pk_bf16_f32 v25, v26, v27\n"
    "v_cvt_pk_bf16_f32 v26, v28, v29\n"
    "v_cvt_pk_bf16_f32 v27, v30, v31\n"
    "v_mfma_f32_16x16x32_bf16 a[4:7], a[196:199], v[4:7], a[4:7]\n"
    "ds_read_b64_tr_b16 a[220:221], %[vb3] offset:9216\n"
    "ds_read_b64_tr_b16 a[222:223], %[vb3] offset:13312\n"
    "v_fma_f32 v32, v32, s43, v217\n"
    "v_fma_f32 v33, v33, s43, v217\n"
    "v_fma_f32 v34, v34, s43, v217\n"
    "v_fma_f32 v35, v35, s43, v217\n"
    "v_fma_f32 v36, v36, s43, v217\n"
    "v_fma_f32 v37, v37, s43, v217\n"
    "v_fma_f32 v38, v38, s43, v217\n"
    "v_mfma_f32_16x16x32_bf16 a[36:39], a[196:199], v[12:15], a[36:39]\n"
    "s_cbranch_vccnz .Lpad18_%=\n"
    ".Lret18_%=:\n"
    "v_fma_f32 v39, v39, s43, v217\n"
    "v_exp_f32 v32, v32\n"
    "v_exp_f32 v33, v33\n"
    "v_exp_f32 v34, v34\n"
    "v_mfma_f32_16x16x32_bf16 a[68:71], a[196:199], v[20:23], a[68:71]\n"
    "s_waitcnt lgkmcnt(2)\n"
    "v_exp_f32 v35, v35\n"
    "v_exp_f32 v36, v36\n"
    "v_exp_f32 v37, v37\n"
    "v_mfma_f32_16x16x32_bf16 a[8:11], a[200:203], v[4:7], a[8:11]\n"
    "s_waitcnt vmcnt(17)\n"
    "s_barrier\n"
    "v_exp_f32 v38, v38\n"
    "v_exp_f32 v39, v39\n"
    "v_cvt_pk_bf16_f32 v32, v32, v33\n"
    "v_cvt_pk_bf16_f32 v33, v34, v35\n"
    "v_cvt_pk_bf16_f32 v34, v36, v37\n"
    "v_cvt_pk_bf16_f32 v35, v38, v39\n"
    "v_mfma_f32_16x16x32_bf16 a[40:43], a[200:203], v[12:15], a[40:43]\n"
    "v_fma_f32 v40, v40, s43, v218\n"
    "v_fma_f32 v41, v41, s43, v218\n"
    "v_fma_f32 v42, v42, s43, v218\n"
    "v_fma_f32 v43, v43, s43, v218\n"
    "v_fma_f32 v44, v44, s43, v218\n"
    "v_fma_f32 v45, v45, s43, v218\n"
    "v_fma_f32 v46, v46, s43, v218\n"
    "v_fma_f32 v47, v47, s43, v218\n"
    "v_mfma_f32_16x16x32_bf16 a[72:75], a[200:203], v[20:23], a[72:75]\n"
    "s_add_u32 s46, s39, 0x3000\n"
    "v_exp_f32 v40, v40\n"
    "v_exp_f32 v41, v41\n"
    "v_exp_f32 v42, v42\n"
    "v_add_u32 %[vb0], 0xffff0000, %[vb0]\n"
    "v_add_u32 %[vb1], 0xffff0000, %[vb1]\n"
    "v_add_u32 %[vb2], 0xffff0000, %[vb2]\n"
    "v_add_u32 %[vb3], 0xffff0000, %[vb3]\n"
    "ds_read_b64_tr_b16 a[192:193], %[vb0] offset:16384\n"
    "ds_read_b64_tr_b16 a[194:195], %[vb0] offset:20480\n"
    "v_mfma_f32_16x16x32_bf16 a[12:15], a[204:207], v[4:7], a[12:15]\n"
    "s_add_u32 m0, s42, 0x14000\n"
    "ds_read_b64_tr_b16 a[196:197], %[vb0] offset:24576\n"
    "ds_read_b64_tr_b16 a[198:199], %[vb0] offset:28672\n"
    "v_exp_f32 v43, v43\n"
    "v_exp_f32 v44, v44\n"
    "v_exp_f32 v45, v45\n"
    "v_exp_f32 v46, v46\n"
    "v_mfma_f32_16x16x32_bf16 a[44:47], a[204:207], v[12:15], a[44:47]\n"
    "buffer_load_dwordx4 %[voo], %[srq], s46 offen nt lds\n"
    "v_exp_f32 v47, v47\n"
    "v_cvt_pk_bf16_f32 v40, v40, v41\n"
    "v_cvt_pk_bf16_f32 v41, v42, v43\n"
    "v_cvt_pk_bf16_f32 v42, v44, v45\n"
    "v_cvt_pk_bf16_f32 v43, v46, v47\n"
    "v_accvgpr_read_b32 v230, a120\n"
    "v_mfma_f32_16x16x32_bf16 a[76:79], a[204:207], v[20:23], a[76:79]\n"
    "v_accvgpr_read_b32 v231, a121\n"
    "v_accvgpr_read_b32 v232, a122\n"
    "v_accvgpr_read_b32 v233, a123\n"
    "v_accvgpr_read_b32 v234, a124\n"
    "v_accvgpr_read_b32 v235, a125\n"
    "v_accvgpr_read_b32 v236, a126\n"
    "v_accvgpr_read_b32 v237, a127\n"
    "v_mfma_f32_16x16x32_bf16 a[16:19], a[208:211], v[4:7], a[16:19]\n"
    "s_add_u32 m0, s42, 0x14380\n"
    "ds_read_b64_tr_b16 a[200:201], %[vb1] offset:16384\n"
    "ds_read_b64_tr_b16 a[202:203], %[vb1] offset:20480\n"
    "v_fma_f32 v230, v230, s43, v216\n"
    "v_fma_f32 v231, v231, s43, v216\n"
    "v_fma_f32 v232, v232, s43, v216\n"
    "v_fma_f32 v233, v233, s43, v216\n"
    "v_fma_f32 v234, v234, s43, v216\n"
    "v_fma_f32 v235, v235, s43, v216\n"
    "v_fma_f32 v236, v236, s43, v216\n"
    "v_mfma_f32_16x16x32_bf16 a[48:51], a[208:211], v[12:15], a[48:51]\n"
    "v_fma_f32 v237, v237, s43, v216\n"
    "v_exp_f32 v230, v230\n"
    "v_exp_f32 v231, v231\n"
    "v_exp_f32 v232, v232\n"
    "v_mfma_f32_16x16x32_bf16 a[80:83], a[208:211], v[20:23], a[80:83]\n"
    "v_exp_f32 v233, v233\n"
    "v_exp_f32 v234, v234\n"
    "v_exp_f32 v235, v235\n"
    "v_mfma_f32_16x16x32_bf16 a[20:23], a[212:215], v[4:7], a[20:23]\n"
    "ds_read_b64_tr_b16 a[204:205], %[vb1] offset:24576\n"
    "ds_read_b64_tr_b16 a[206:207], %[vb1] offset:28672\n"
    "v_exp_f32 v236, v236\n"
    "v_exp_f32 v237, v237\n"
    "v_cvt_pk_bf16_f32 v28, v230, v231\n"
    "v_cvt_pk_bf16_f32 v29, v232, v233\n"
    "v_cvt_pk_bf16_f32 v30, v234, v235\n"
    "v_cvt_pk_bf16_f32 v31, v236, v237\n"
    "v_mfma_f32_16x16x32_bf16 a[52:55], a[212:215], v[12:15], a[52:55]\n"
    "buffer_load_dwordx4 %[voo], %[srq], s46 offen offset:128 nt lds\n"
    "v_accvgpr_read_b32 v230, a128\n"
    "v_accvgpr_read_b32 v231, a129\n"
    "v_accvgpr_read_b32 v232, a130\n"
    "v_accvgpr_read_b32 v233, a131\n"
    "v_accvgpr_read_b32 v234, a132\n"
    "v_accvgpr_read_b32 v235, a133\n"
    "v_accvgpr_read_b32 v236, a134\n"
    "v_accvgpr_read_b32 v237, a135\n"
    "v_mfma_f32_16x16x32_bf16 a[84:87], a[212:215], v[20:23], a[84:87]\n"
    "v_fma_f32 v230, v230, s43, v217\n"
    "v_fma_f32 v231, v231, s43, v217\n"
    "v_fma_f32 v232, v232, s43, v217\n"
    "v_fma_f32 v233, v233, s43, v217\n"
    "v_fma_f32 v234, v234, s43, v217\n"
    "v_fma_f32 v235, v235, s43, v217\n"
    "v_fma_f32 v236, v236, s43, v217\n"
    "v_mfma_f32_16x16x32_bf16 a[24:27], a[216:219], v[4:7], a[24:27]\n"
    "s_add_u32 m0, s42, 0x14700\n"
    "ds_read_b64_tr_b16 a[208:209], %[vb2] offset:16384\n"
    "ds_read_b64_tr_b16 a[210:211], %[vb2] offset:20480\n"
    "v_fma_f32 v237, v237, s43, v217\n"
    "v_exp_f32 v230, v230\n"
    "v_exp_f32 v231, v231\n"
    "v_exp_f32 v232, v232\n"
    "v_mfma_f32_16x16x32_bf16 a[56:59], a[216:219], v[12:15], a[56:59]\n"
    "v_exp_f32 v233, v233\n"
    "v_exp_f32 v234, v234\n"
    "v_exp_f32 v235, v235\n"
    "v_mfma_f32_16x16x32_bf16 a[88:91], a[216:219], v[20:23], a[88:91]\n"
    "s_waitcnt lgkmcnt(2)\n"
    "v_exp_f32 v236, v236\n"
    "v_exp_f32 v237, v237\n"
    "v_cvt_pk_bf16_f32 v36, v230, v231\n"
    "v_cvt_pk_bf16_f32 v37, v232, v233\n"
    "v_cvt_pk_bf16_f32 v38, v234, v235\n"
    "v_cvt_pk_bf16_f32 v39, v236, v237\n"
    "v_mfma_f32_16x16x32_bf16 a[28:31], a[220:223], v[4:7], a[28:31]\n"
    "buffer_load_dwordx4 %[voo], %[srq], s46 offen offset:256 nt lds\n"
    "v_accvgpr_read_b32 v230, a136\n"
    "v_accvgpr_read_b32 v231, a137\n"
    "v_accvgpr_read_b32 v232, a138\n"
    "v_accvgpr_read_b32 v233, a139\n"
    "v_accvgpr_read_b32 v234, a140\n"
    "v_accvgpr_read_b32 v235, a141\n"
    "v_accvgpr_read_b32 v236, a142\n"
    "v_mfma_f32_16x16x32_bf16 a[60:63], a[220:223], v[12:15], a[60:63]\n"
    "v_accvgpr_read_b32 v237, a143\n"
    "v_fma_f32 v230, v230, s43, v218\n"
    "v_fma_f32 v231, v231, s43, v218\n"
    "v_fma_f32 v232, v232, s43, v218\n"
    "v_fma_f32 v233, v233, s43, v218\n"
    "v_fma_f32 v234, v234, s43, v218\n"
    "v_fma_f32 v235, v235, s43, v218\n"
    "v_mfma_f32_16x16x32_bf16 a[92:95], a[220:223], v[20:23], a[92:95]\n"
    "v_fma_f32 v236, v236, s43, v218\n"
    "v_fma_f32 v237, v237, s43, v218\n"
    "v_exp_f32 v230, v230\n"
    "v_exp_f32 v231, v231\n"
    "v_mfma_f32_16x16x32_bf16 a[228:231], a[224:227], v[4:7], a[228:231]\n"
    "s_add_u32 m0, s42, 0x14a80\n"
    "ds_read_b64_tr_b16 a[212:213], %[vb2] offset:24576\n"
    "ds_read_b64_tr_b16 a[214:215], %[vb2] offset:28672\n"
    "v_exp_f32 v232, v232\n"
    "v_exp_f32 v233, v233\n"
    "v_exp_f32 v234, v234\n"
    "v_exp_f32 v235, v235\n"
    "v_mfma_f32_16x16x32_bf16 a[232:235], a[224:227], v[12:15], a[232:235]\n"
    "ds_read_b64_tr_b16 a[216:217], %[vb3] offset:16384\n"
    "ds_read_b64_tr_b16 a[218:219], %[vb3] offset:20480\n"
    "v_exp_f32 v236, v236\n"
    "v_exp_f32 v237, v237\n"
    "v_cvt_pk_bf16_f32 v44, v230, v231\n"
    "v_cvt_pk_bf16_f32 v45, v232, v233\n"
    "v_cvt_pk_bf16_f32 v46, v234, v235\n"
    "v_cvt_pk_bf16_f32 v47, v236, v237\n"
    "v_mfma_f32_16x16x32_bf16 a[236:239], a[224:227], v[20:23], a[236:239]\n"
    "v_fma_f32 v48, v48, s43, v216\n"
    "v_fma_f32 v49, v49, s43, v216\n"
    "v_fma_f32 v50, v50, s43, v216\n"
    "v_mfma_f32_16x16x32_bf16 a[0:3], a[192:195], v[24:27], a[0:3]\n"
    "buffer_load_dwordx4 %[voo], %[srq], s46 offen offset:384 nt lds\n"
    "v_fma_f32 v51, v51, s43, v216\n"
    "v_fma_f32 v52, v52, s43, v216\n"
    "v_fma_f32 v53, v53, s43, v216\n"
    "v_fma_f32 v54, v54, s43, v216\n"
    "v_mfma_f32_16x16x32_bf16 a[0:3], a[196:199], v[28:31], a[0:3]\n"
    "v_fma_f32 v55, v55, s43, v216\n"
    "v_exp_f32 v48, v48\n"
    "v_mfma_f32_16x16x32_bf16 a[32:35], a[192:195], v[32:35], a[32:35]\n"
    "v_exp_f32 v49, v49\n"
    "v_exp_f32 v50, v50\n"
    "v_mfma_f32_16x16x32_bf16 a[32:35], a[196:199], v[36:39], a[32:35]\n"
    "ds_read_b64_tr_b16 a[220:221], %[vb3] offset:24576\n"
    "ds_read_b64_tr_b16 a[222:223], %[vb3] offset:28672\n"
    "v_exp_f32 v51, v51\n"
    "v_mfma_f32_16x16x32_bf16 a[64:67], a[192:195], v[40:43], a[64:67]\n"
    "ds_read_b64_tr_b16 a[192:193], %[vb0] offset:17408\n"
    "ds_read_b64_tr_b16 a[194:195], %[vb0] offset:21504\n"
    "v_exp_f32 v52, v52\n"
    "v_exp_f32 v53, v53\n"
    "v_mfma_f32_16x16x32_bf16 a[64:67], a[196:199], v[44:47], a[64:67]\n"
    "s_waitcnt vmcnt(21)\n"
    "v_exp_f32 v54, v54\n"
    "v_exp_f32 v55, v55\n"
    "v_mfma_f32_16x16x32_bf16 a[4:7], a[200:203], v[24:27], a[4:7]\n"
    "v_cvt_pk_bf16_f32 v48, v48, v49\n"
    "v_cvt_pk_bf16_f32 v49, v50, v51\n"
    "v_cvt_pk_bf16_f32 v50, v52, v53\n"
    "v_cvt_pk_bf16_f32 v51, v54, v55\n"
    "v_mfma_f32_16x16x32_bf16 a[4:7], a[204:207], v[28:31], a[4:7]\n"
    "v_fma_f32 v56, v56, s43, v217\n"
    "v_fma_f32 v57, v57, s43, v217\n"
    "v_fma_f32 v58, v58, s43, v217\n"
    "v_fma_f32 v59, v59, s43, v217\n"
    "v_mfma_f32_16x16x32_bf16 a[36:39], a[200:203], v[32:35], a[36:39]\n"
    "v_fma_f32 v60, v60, s43, v217\n"
    "v_fma_f32 v61, v61, s43, v217\n"
    "v_fma_f32 v62, v62, s43, v217\n"
    "v_mfma_f32_16x16x32_bf16 a[36:39], a[204:207], v[36:39], a[36:39]\n"
    "s_cbranch_vccnz .Lpad19_%=\n"
    ".Lret19_%=:\n"
    "ds_read_b64_tr_b16 a[196:197], %[vb0] offset:25600\n"
    "ds_read_b64_tr_b16 a[198:199], %[vb0] offset:29696\n"
    "v_fma_f32 v63, v63, s43, v217\n"
    "v_add_u32 %[qb0], 0x10000, %[qb0]\n"
    "v_exp_f32 v56, v56\n"
    "v_mfma_f32_16x16x32_bf16 a[68:71], a[200:203], v[40:43], a[68:71]\n"
    "ds_read_b64_tr_b16 a[200:201], %[vb1] offset:17408\n"
    "ds_read_b64_tr_b16 a[202:203], %[vb1] offset:21504\n"
    "v_exp_f32 v57, v57\n"
    "v_exp_f32 v58, v58\n"
    "v_mfma_f32_16x16x32_bf16 a[68:71], a[204:207], v[44:47], a[68:71]\n"
    "s_waitcnt lgkmcnt(4)\n"
    "v_exp_f32 v59, v59\n"
    "v_add_u32 %[qb1], 0x10000, %[qb1]\n"
    "v_exp_f32 v60, v60\n"
    "v_mfma_f32_16x16x32_bf16 a[8:11], a[208:211], v[24:27], a[8:11]\n"
    "ds_read_b128 a[96:99], %[qb0] offset:32768\n"
    "ds_read_b128 a[100:103], %[qb1] offset:32768\n"
    "v_exp_f32 v61, v61\n"
    "v_mfma_f32_16x16x32_bf16 a[8:11], a[212:215], v[28:31], a[8:11]\n"
    "ds_read_b128 a[104:107], %[qb0] offset:33792\n"
    "v_exp_f32 v62, v62\n"
    "v_exp_f32 v63, v63\n"
    "v_cvt_pk_bf16_f32 v56, v56, v57\n"
    "v_mfma_f32_16x16x32_bf16 a[40:43], a[208:211], v[32:35], a[40:43]\n"
    "ds_read_b128 a[108:111], %[qb1] offset:33792\n"
    "v_cvt_pk_bf16_f32 v57, v58, v59\n"
    "v_cvt_pk_bf16_f32 v58, v60, v61\n"
    "v_cvt_pk_bf16_f32 v59, v62, v63\n"
    "v_mfma_f32_16x16x32_bf16 a[40:43], a[212:215], v[36:39], a[40:43]\n"
    "ds_read_b64_tr_b16 a[204:205], %[vb1] offset:25600\n"
    "ds_read_b64_tr_b16 a[206:207], %[vb1] offset:29696\n"
    "v_fma_f32 v64, v64, s43, v218\n"
    "v_fma_f32 v65, v65, s43, v218\n"
    "v_fma_f32 v66, v66, s43, v218\n"
    "v_fma_f32 v67, v67, s43, v218\n"
    "v_mfma_f32_16x16x32_bf16 a[72:75], a[208:211], v[40:43], a[72:75]\n"
    "ds_read_b64_tr_b16 a[208:209], %[vb2] offset:17408\n"
    "ds_read_b64_tr_b16 a[210:211], %[vb2] offset:21504\n"
    "v_fma_f32 v68, v68, s43, v218\n"
    "v_fma_f32 v69, v69, s43, v218\n"
    "v_fma_f32 v70, v70, s43, v218\n"
    "v_fma_f32 v71, v71, s43, v218\n"
    "v_mfma_f32_16x16x32_bf16 a[72:75], a[212:215], v[44:47], a[72:75]\n"
    "ds_read_b128 a[112:115], %[qb0] offset:34816\n"
    "v_exp_f32 v64, v64\n"
    "v_mfma_f32_16x16x32_bf16 a[12:15], a[216:219], v[24:27], a[12:15]\n"
    "ds_read_b128 a[116:119], %[qb1] offset:34816\n"
    "v_exp_f32 v65, v65\n"
    "v_exp_f32 v66, v66\n"
    "v_mfma_f32_16x16x32_bf16 a[12:15], a[220:223], v[28:31], a[12:15]\n"
    "ds_read_b128 a[120:123], %[qb0] offset:35840\n"
    "v_exp_f32 v67, v67\n"
    "v_exp_f32 v68, v68\n"
    "v_mfma_f32_16x16x32_bf16 a[44:47], a[216:219], v[32:35], a[44:47]\n"
    "s_waitcnt lgkmcnt(8)\n"
    "ds_read_b128 a[124:127], %[qb1] offset:35840\n"
    "v_exp_f32 v69, v69\n"
    "v_exp_f32 v70, v70\n"
    "v_mfma_f32_16x16x32_bf16 a[44:47], a[220:223], v[36:39], a[44:47]\n"
    "ds_read_b64_tr_b16 a[212:213], %[vb2] offset:25600\n"
    "ds_read_b64_tr_b16 a[214:215], %[vb2] offset:29696\n"
    "v_exp_f32 v71, v71\n"
    "v_cvt_pk_bf16_f32 v64, v64, v65\n"
    "v_mfma_f32_16x16x32_bf16 a[76:79], a[216:219], v[40:43], a[76:79]\n"
    "ds_read_b64_tr_b16 a[216:217], %[vb3] offset:17408\n"
    "ds_read_b64_tr_b16 a[218:219], %[vb3] offset:21504\n"
    "v_cvt_pk_bf16_f32 v65, v66, v67\n"
    "v_cvt_pk_bf16_f32 v66, v68, v69\n"
    "v_cvt_pk_bf16_f32 v67, v70, v71\n"
    "v_accvgpr_read_b32 v230, a144\n"
    "v_mfma_f32_16x16x32_bf16 a[76:79], a[220:223], v[44:47], a[76:79]\n"
    "v_accvgpr_read_b32 v231, a145\n"
    "v_accvgpr_read_b32 v232, a146\n"
    "v_accvgpr_read_b32 v233, a147\n"
    "v_mfma_f32_16x16x32_bf16 a[16:19], a[192:195], v[24:27], a[16:19]\n"
    "v_accvgpr_read_b32 v234, a148\n"
    "v_accvgpr_read_b32 v235, a149\n"
    "v_accvgpr_read_b32 v236, a150\n"
    "v_accvgpr_read_b32 v237, a151\n"
    "v_mfma_f32_16x16x32_bf16 a[16:19], a[196:199], v[28:31], a[16:19]\n"
    "v_fma_f32 v230, v230, s43, v216\n"
    "v_fma_f32 v231, v231, s43, v216\n"
    "v_fma_f32 v232, v232, s43, v216\n"
    "v_fma_f32 v233, v233, s43, v216\n"
    "v_mfma_f32_16x16x32_bf16 a[48:51], a[192:195], v[32:35], a[48:51]\n"
    "v_fma_f32 v234, v234, s43, v216\n"
    "v_fma_f32 v235, v235, s43, v216\n"
    "v_fma_f32 v236, v236, s43, v216\n"
    "v_mfma_f32_16x16x32_bf16 a[48:51], a[196:199], v[36:39], a[48:51]\n"
    "ds_read_b64_tr_b16 a[220:221], %[vb3] offset:25600\n"
    "ds_read_b64_tr_b16 a[222:223], %[vb3] offset:29696\n"
    "v_fma_f32 v237, v237, s43, v216\n"
    "v_exp_f32 v230, v230\n"
    "v_mfma_f32_16x16x32_bf16 a[80:83], a[192:195], v[40:43], a[80:83]\n"
    "s_waitcnt vmcnt(16)\n"
    "s_barrier\n"
    "s_waitcnt lgkmcnt(8)\n"
    "ds_read_b64_tr_b16 a[192:193], %[vb0] offset:49152\n"
    "v_exp_f32 v231, v231\n"
    "v_exp_f32 v232, v232\n"
    "v_mfma_f32_16x16x32_bf16 a[80:83], a[196:199], v[44:47], a[80:83]\n"
    "ds_read_b64_tr_b16 a[194:195], %[vb0] offset:53248\n"
    "v_exp_f32 v233, v233\n"
    "v_exp_f32 v234, v234\n"
    "v_mfma_f32_16x16x32_bf16 a[20:23], a[200:203], v[24:27], a[20:23]\n"
    "s_add_u32 s46, s39, 0x2000\n"
    "v_exp_f32 v235, v235\n"
    "v_mfma_f32_16x16x32_bf16 a[20:23], a[204:207], v[28:31], a[20:23]\n"
    "s_add_u32 m0, s42, 0x10000\n"
    "v_exp_f32 v236, v236\n"
    "v_exp_f32 v237, v237\n"
    "v_cvt_pk_bf16_f32 v52, v230, v231\n"
    "v_mfma_f32_16x16x32_bf16 a[52:55], a[200:203], v[32:35], a[52:55]\n"
    "buffer_load_dwordx4 %[voe], %[srq], s46 offen nt lds\n"
    "v_cvt_pk_bf16_f32 v53, v232, v233\n"
    "v_cvt_pk_bf16_f32 v54, v234, v235\n"
    "v_cvt_pk_bf16_f32 v55, v236, v237\n"
    "v_mfma_f32_16x16x32_bf16 a[52:55], a[204:207], v[36:39], a[52:55]\n"
    "ds_read_b64_tr_b16 a[196:197], %[vb0] offset:57344\n"
    "ds_read_b64_tr_b16 a[198:199], %[vb0] offset:61440\n"
    "v_accvgpr_read_b32 v230, a152\n"
    "v_accvgpr_read_b32 v231, a153\n"
    "v_accvgpr_read_b32 v232, a154\n"
    "v_accvgpr_read_b32 v233, a155\n"
    "v_mfma_f32_16x16x32_bf16 a[84:87], a[200:203], v[40:43], a[84:87]\n"
    "s_add_u32 m0, s42, 0x10380\n"
    "ds_read_b64_tr_b16 a[200:201], %[vb1] offset:49152\n"
    "ds_read_b64_tr_b16 a[202:203], %[vb1] offset:53248\n"
    "v_accvgpr_read_b32 v234, a156\n"
    "v_accvgpr_read_b32 v235, a157\n"
    "v_accvgpr_read_b32 v236, a158\n"
    "v_accvgpr_read_b32 v237, a159\n"
    "v_mfma_f32_16x16x32_bf16 a[84:87], a[204:207], v[44:47], a[84:87]\n"
    "v_fma_f32 v230, v230, s43, v217\n"
    "v_fma_f32 v231, v231, s43, v217\n"
    "v_fma_f32 v232, v232, s43, v217\n"
    "v_mfma_f32_16x16x32_bf16 a[24:27], a[208:211], v[24:27], a[24:27]\n"
    "s_waitcnt lgkmcnt(4)\n"
    "v_fma_f32 v233, v233, s43, v217\n"
    "v_fma_f32 v234, v234, s43, v217\n"
    "v_fma_f32 v235, v235, s43, v217\n"
    "v_fma_f32 v236, v236, s43, v217\n"
    "v_mfma_f32_16x16x32_bf16 a[24:27], a[212:215], v[28:31], a[24:27]\n"
    "buffer_load_dwordx4 %[voe], %[srq], s46 offen offset:128 nt lds\n"
    "v_fma_f32 v237, v237, s43, v217\n"
    "v_exp_f32 v230, v230\n"
    "v_mfma_f32_16x16x32_bf16 a[56:59], a[208:211], v[32:35], a[56:59]\n"
    "v_exp_f32 v231, v231\n"
    "v_exp_f32 v232, v232\n"
    "v_mfma_f32_16x16x32_bf16 a[56:59], a[212:215], v[36:39], a[56:59]\n"
    "ds_read_b64_tr_b16 a[204:205], %[vb1] offset:57344\n"
    "ds_read_b64_tr_b16 a[206:207], %[vb1] offset:61440\n"
    "v_exp_f32 v233, v233\n"
    "v_mfma_f32_16x16x32_bf16 a[88:91], a[208:211], v[40:43], a[88:91]\n"
    "s_add_u32 m0, s42, 0x10700\n"
    "ds_read_b64_tr_b16 a[208:209], %[vb2] offset:49152\n"
    "ds_read_b64_tr_b16 a[210:211], %[vb2] offset:53248\n"
    "v_exp_f32 v234, v234\n"
    "v_exp_f32 v235, v235\n"
    "v_mfma_f32_16x16x32_bf16 a[88:91], a[212:215], v[44:47], a[88:91]\n"
    "s_cbranch_vccnz .Lpad20_%=\n"
    ".Lret20_%=:\n"
    "v_exp_f32 v236, v236\n"
    "v_exp_f32 v237, v237\n"
    "v_mfma_f32_16x16x32_bf16 a[28:31], a[216:219], v[24:27], a[28:31]\n"
    "v_cvt_pk_bf16_f32 v60, v230, v231\n"
    "v_cvt_pk_bf16_f32 v61, v232, v233\n"
    "v_cvt_pk_bf16_f32 v62, v234, v235\n"
    "v_cvt_pk_bf16_f32 v63, v236, v237\n"
    "v_mfma_f32_16x16x32_bf16 a[28:31], a[220:223], v[28:31], a[28:31]\n"
    "buffer_load_dwordx4 %[voe], %[srq], s46 offen offset:256 nt lds\n"
    "v_accvgpr_read_b32 v230, a160\n"
    "v_accvgpr_read_b32 v231, a161\n"
    "v_accvgpr_read_b32 v232, a162\n"
    "v_accvgpr_read_b32 v233, a163\n"
    "v_mfma_f32_16x16x32_bf16 a[60:63], a[216:219], v[32:35], a[60:63]\n"
    "v_accvgpr_read_b32 v234, a164\n"
    "v_accvgpr_read_b32 v235, a165\n"
    "v_accvgpr_read_b32 v236, a166\n"
    "v_mfma_f32_16x16x32_bf16 a[60:63], a[220:223], v[36:39], a[60:63]\n"
    "v_accvgpr_read_b32 v237, a167\n"
    "v_fma_f32 v230, v230, s43, v218\n"
    "v_fma_f32 v231, v231, s43, v218\n"
    "v_fma_f32 v232, v232, s43, v218\n"
    "v_mfma_f32_16x16x32_bf16 a[92:95], a[216:219], v[40:43], a[92:95]\n"
    "s_add_u32 m0, s42, 0x10a80\n"
    "v_fma_f32 v233, v233, s43, v218\n"
    "v_fma_f32 v234, v234, s43, v218\n"
    "v_fma_f32 v235, v235, s43, v218\n"
    "v_mfma_f32_16x16x32_bf16 a[92:95], a[220:223], v[44:47], a[92:95]\n"
    "v_fma_f32 v236, v236, s43, v218\n"
    "v_fma_f32 v237, v237, s43, v218\n"
    "v_exp_f32 v230, v230\n"
    "v_mfma_f32_16x16x32_bf16 a[228:231], a[224:227], v[24:27], a[228:231]\n"
    "v_exp_f32 v231, v231\n"
    "v_mfma_f32_16x16x32_bf16 a[228:231], a[224:227], v[28:31], a[228:231]\n"
    "buffer_load_dwordx4 %[voe], %[srq], s46 offen offset:384 nt lds\n"
    "v_exp_f32 v232, v232\n"
    "v_exp_f32 v233, v233\n"
    "v_mfma_f32_16x16x32_bf16 a[232:235], a[224:227], v[32:35], a[232:235]\n"
    "v_exp_f32 v234, v234\n"
    "v_exp_f32 v235, v235\n"
    "v_mfma_f32_16x16x32_bf16 a[232:235], a[224:227], v[36:39], a[232:235]\n"
    "ds_read_b64_tr_b16 a[212:213], %[vb2] offset:57344\n"
    "ds_read_b64_tr_b16 a[214:215], %[vb2] offset:61440\n"
    "v_exp_f32 v236, v236\n"
    "v_exp_f32 v237, v237\n"
    "v_mfma_f32_16x16x32_bf16 a[236:239], a[224:227], v[40:43], a[236:239]\n"
    "s_add_u32 s46, s38, 0x28000\n"
    "ds_read_b64_tr_b16 a[216:217], %[vb3] offset:49152\n"
    "ds_read_b64_tr_b16 a[218:219], %[vb3] offset:53248\n"
    "v_cvt_pk_bf16_f32 v68, v230, v231\n"
    "v_cvt_pk_bf16_f32 v69, v232, v233\n"
    "v_cvt_pk_bf16_f32 v70, v234, v235\n"
    "v_cvt_pk_bf16_f32 v71, v236, v237\n"
    "v_mfma_f32_16x16x32_bf16 a[236:239], a[224:227], v[44:47], a[236:239]\n"
    "s_add_u32 m0, s42, 0x1c000\n"
    "v_fma_f32 v72, v72, s43, v216\n"
    "v_fma_f32 v73, v73, s43, v216\n"
    "v_fma_f32 v74, v74, s43, v216\n"
    "v_mfma_f32_16x16x32_bf16 a[0:3], a[192:195], v[48:51], a[0:3]\n"
    "s_waitcnt lgkmcnt(4)\n"
    "v_fma_f32 v75, v75, s43, v216\n"
    "v_fma_f32 v76, v76, s43, v216\n"
    "v_fma_f32 v77, v77, s43, v216\n"
    "v_mfma_f32_16x16x32_bf16 a[0:3], a[196:199], v[52:55], a[0:3]\n"
    "buffer_load_dwordx4 %[vov], %[srv], s46 offen sc0 sc1 lds\n"
    "v_fma_f32 v78, v78, s43, v216\n"
    "v_fma_f32 v79, v79, s43, v216\n"
    "v_mfma_f32_16x16x32_bf16 a[32:35], a[192:195], v[56:59], a[32:35]\n"
    "v_exp_f32 v72, v72\n"
    "v_exp_f32 v73, v73\n"
    "v_mfma_f32_16x16x32_bf16 a[32:35], a[196:199], v[60:63], a[32:35]\n"
    "ds_read_b64_tr_b16 a[220:221], %[vb3] offset:57344\n"
    "ds_read_b64_tr_b16 a[222:223], %[vb3] offset:61440\n"
    "v_exp_f32 v74, v74\n"
    "v_mfma_f32_16x16x32_bf16 a[64:67], a[192:195], v[64:67], a[64:67]\n"
    "s_add_u32 m0, s42, 0x1c380\n"
    "ds_read_b64_tr_b16 a[192:193], %[vb0] offset:50176\n"
    "ds_read_b64_tr_b16 a[194:195], %[vb0] offset:54272\n"
    "v_exp_f32 v75, v75\n"
    "v_exp_f32 v76, v76\n"
    "v_mfma_f32_16x16x32_bf16 a[64:67], a[196:199], v[68:71], a[64:67]\n"
    "v_exp_f32 v77, v77\n"
    "v_mfma_f32_16x16x32_bf16 a[4:7], a[200:203], v[48:51], a[4:7]\n"
    "v_exp_f32 v78, v78\n"
    "v_exp_f32 v79, v79\n"
    "v_mfma_f32_16x16x32_bf16 a[4:7], a[204:207], v[52:55], a[4:7]\n"
    "buffer_load_dwordx4 %[vov], %[srv], s46 offen offset:128 sc0 sc1 lds\n"
    "v_cvt_pk_bf16_f32 v72, v72, v73\n"
    "v_cvt_pk_bf16_f32 v73, v74, v75\n"
    "v_cvt_pk_bf16_f32 v74, v76, v77\n"
    "v_cvt_pk_bf16_f32 v75, v78, v79\n"
    "v_mfma_f32_16x16x32_bf16 a[36:39], a[200:203], v[56:59], a[36:39]\n"
    "v_fma_f32 v80, v80, s43, v217\n"
    "v_fma_f32 v81, v81, s43, v217\n"
    "v_fma_f32 v82, v82, s43, v217\n"
    "v_mfma_f32_16x16x32_bf16 a[36:39], a[204:207], v[60:63], a[36:39]\n"
    "ds_read_b64_tr_b16 a[196:197], %[vb0] offset:58368\n"
    "ds_read_b64_tr_b16 a[198:199], %[vb0] offset:62464\n"
    "v_fma_f32 v83, v83, s43, v217\n"
    "v_fma_f32 v84, v84, s43, v217\n"
    "v_fma_f32 v85, v85, s43, v217\n"
    "v_mfma_f32_16x16x32_bf16 a[68:71], a[200:203], v[64:67], a[68:71]\n"
    "s_add_u32 s46, s38, 0x29000\n"
    "ds_read_b64_tr_b16 a[200:201], %[vb1] offset:50176\n"
    "ds_read_b64_tr_b16 a[202:203], %[vb1] offset:54272\n"
    "v_fma_f32 v86, v86, s43, v217\n"
    "v_fma_f32 v87, v87, s43, v217\n"
    "v_mfma_f32_16x16x32_bf16 a[68:71], a[204:207], v[68:71], a[68:71]\n"
    "s_add_u32 m0, s42, 0x1c800\n"
    "v_exp_f32 v80, v80\n"
    "v_exp_f32 v81, v81\n"
    "v_mfma_f32_16x16x32_bf16 a[8:11], a[208:211], v[48:51], a[8:11]\n"
    "s_waitcnt lgkmcnt(4)\n"
    "v_exp_f32 v82, v82\n"
    "v_mfma_f32_16x16x32_bf16 a[8:11], a[212:215], v[52:55], a[8:11]\n"
    "buffer_load_dwordx4 %[vov], %[srv], s46 offen sc0 sc1 lds\n"
    "v_exp_f32 v83, v83\n"
    "v_exp_f32 v84, v84\n"
    "v_mfma_f32_16x16x32_bf16 a[40:43], a[208:211], v[56:59], a[40:43]\n"
    "v_exp_f32 v85, v85\n"
    "v_mfma_f32_16x16x32_bf16 a[40:43], a[212:215], v[60:63], a[40:43]\n"
    "ds_read_b64_tr_b16 a[204:205], %[vb1] offset:58368\n"
    "ds_read_b64_tr_b16 a[206:207], %[vb1] offset:62464\n"
    "v_exp_f32 v86, v86\n"
    "v_exp_f32 v87, v87\n"
    "v_mfma_f32_16x16x32_bf16 a[72:75], a[208:211], v[64:67], a[72:75]\n"
    "s_add_u32 m0, s42, 0x1cb80\n"
    "ds_read_b64_tr_b16 a[208:209], %[vb2] offset:50176\n"
    "ds_read_b64_tr_b16 a[210:211], %[vb2] offset:54272\n"
    "v_cvt_pk_bf16_f32 v80, v80, v81\n"
    "v_cvt_pk_bf16_f32 v81, v82, v83\n"
    "v_cvt_pk_bf16_f32 v82, v84, v85\n"
    "v_cvt_pk_bf16_f32 v83, v86, v87\n"
    "v_mfma_f32_16x16x32_bf16 a[72:75], a[212:215], v[68:71], a[72:75]\n"
    "v_fma_f32 v88, v88, s43, v218\n"
    "v_fma_f32 v89, v89, s43, v218\n"
    "v_fma_f32 v90, v90, s43, v218\n"
    "v_mfma_f32_16x16x32_bf16 a[12:15], a[216:219], v[48:51], a[12:15]\n"
    "v_fma_f32 v91, v91, s43, v218\n"
    "v_fma_f32 v92, v92, s43, v218\n"
    "v_fma_f32 v93, v93, s43, v218\n"
    "v_mfma_f32_16x16x32_bf16 a[12:15], a[220:223], v[52:55], a[12:15]\n"
    "s_cbranch_vccnz .Lpad21_%=\n"
    ".Lret21_%=:\n"
    "buffer_load_dwordx4 %[vov], %[srv], s46 offen offset:128 sc0 sc1 lds\n"
    "v_fma_f32 v94, v94, s43, v218\n"
    "v_fma_f32 v95, v95, s43, v218\n"
    "v_mfma_f32_16x16x32_bf16 a[44:47], a[216:219], v[56:59], a[44:47]\n"
    "v_exp_f32 v88, v88\n"
    "v_exp_f32 v89, v89\n"
    "v_mfma_f32_16x16x32_bf16 a[44:47], a[220:223], v[60:63], a[44:47]\n"
    "ds_read_b64_tr_b16 a[212:213], %[vb2] offset:58368\n"
    "ds_read_b64_tr_b16 a[214:215], %[vb2] offset:62464\n"
    "v_exp_f32 v90, v90\n"
    "v_mfma_f32_16x16x32_bf16 a[76:79], a[216:219], v[64:67], a[76:79]\n"
    "ds_read_b64_tr_b16 a[216:217], %[vb3] offset:50176\n"
    "ds_read_b64_tr_b16 a[218:219], %[vb3] offset:54272\n"
    "v_exp_f32 v91, v91\n"
    "v_exp_f32 v92, v92\n"
    "v_mfma_f32_16x16x32_bf16 a[76:79], a[220:223], v[68:71], a[76:79]\n"
    "v_exp_f32 v93, v93\n"
    "v_mfma_f32_16x16x32_bf16 a[16:19], a[192:195], v[48:51], a[16:19]\n"
    "s_waitcnt lgkmcnt(4)\n"
    "v_exp_f32 v94, v94\n"
    "v_exp_f32 v95, v95\n"
    "v_mfma_f32_16x16x32_bf16 a[16:19], a[196:199], v[52:55], a[16:19]\n"
    "v_cvt_pk_bf16_f32 v88, v88, v89\n"
    "v_cvt_pk_bf16_f32 v89, v90, v91\n"
    "v_cvt_pk_bf16_f32 v90, v92, v93\n"
    "v_cvt_pk_bf16_f32 v91, v94, v95\n"
    "v_mfma_f32_16x16x32_bf16 a[48:51], a[192:195], v[56:59], a[48:51]\n"
    "v_fma_f32 v96, v96, s43, v216\n"
    "v_fma_f32 v97, v97, s43, v216\n"
    "v_fma_f32 v98, v98, s43, v216\n"
    "v_mfma_f32_16x16x32_bf16 a[48:51], a[196:199], v[60:63], a[48:51]\n"
    "ds_read_b64_tr_b16 a[220:221], %[vb3] offset:58368\n"
    "ds_read_b64_tr_b16 a[222:223], %[vb3] offset:62464\n"
    "v_fma_f32 v99, v99, s43, v216\n"
    "v_fma_f32 v100, v100, s43, v216\n"
    "v_fma_f32 v101, v101, s43, v216\n"
    "v_mfma_f32_16x16x32_bf16 a[80:83], a[192:195], v[64:67], a[80:83]\n"
    "s_waitcnt vmcnt(18)\n"
    "s_barrier\n"
    "ds_read_b64_tr_b16 a[192:193], %[vb0] offset:32768\n"
    "ds_read_b64_tr_b16 a[194:195], %[vb0] offset:36864\n"
    "v_fma_f32 v102, v102, s43, v216\n"
    "v_fma_f32 v103, v103, s43, v216\n"
    "v_mfma_f32_16x16x32_bf16 a[80:83], a[196:199], v[68:71], a[80:83]\n"
    "v_exp_f32 v96, v96\n"
    "v_exp_f32 v97, v97\n"
    "v_mfma_f32_16x16x32_bf16 a[20:23], a[200:203], v[48:51], a[20:23]\n"
    "s_add_u32 s46, s38, 0x100\n"
    "v_exp_f32 v98, v98\n"
    "v_mfma_f32_16x16x32_bf16 a[20:23], a[204:207], v[52:55], a[20:23]\n"
    "s_add_u32 m0, s42, 0x4000\n"
    "v_exp_f32 v99, v99\n"
    "v_exp_f32 v100, v100\n"
    "v_mfma_f32_16x16x32_bf16 a[52:55], a[200:203], v[56:59], a[52:55]\n"
    "buffer_load_dwordx4 %[vov], %[srv], s46 offen sc0 sc1 lds\n"
    "v_exp_f32 v101, v101\n"
    "v_mfma_f32_16x16x32_bf16 a[52:55], a[204:207], v[60:63], a[52:55]\n"
    "ds_read_b64_tr_b16 a[196:197], %[vb0] offset:40960\n"
    "ds_read_b64_tr_b16 a[198:199], %[vb0] offset:45056\n"
    "v_exp_f32 v102, v102\n"
    "v_exp_f32 v103, v103\n"
    "v_mfma_f32_16x16x32_bf16 a[84:87], a[200:203], v[64:67], a[84:87]\n"
    "s_add_u32 m0, s42, 0x4380\n"
    "ds_read_b64_tr_b16 a[200:201], %[vb1] offset:32768\n"
    "ds_read_b64_tr_b16 a[202:203], %[vb1] offset:36864\n"
    "v_cvt_pk_bf16_f32 v96, v96, v97\n"
    "v_cvt_pk_bf16_f32 v97, v98, v99\n"
    "v_cvt_pk_bf16_f32 v98, v100, v101\n"
    "v_cvt_pk_bf16_f32 v99, v102, v103\n"
    "v_mfma_f32_16x16x32_bf16 a[84:87], a[204:207], v[68:71], a[84:87]\n"
    "v_fma_f32 v104, v104, s43, v217\n"
    "v_fma_f32 v105, v105, s43, v217\n"
    "v_fma_f32 v106, v106, s43, v217\n"
    "v_mfma_f32_16x16x32_bf16 a[24:27], a[208:211], v[48:51], a[24:27]\n"
    "s_waitcnt lgkmcnt(4)\n"
    "v_fma_f32 v107, v107, s43, v217\n"
    "v_fma_f32 v108, v108, s43, v217\n"
    "v_fma_f32 v109, v109, s43, v217\n"
    "v_mfma_f32_16x16x32_bf16 a[24:27], a[212:215], v[52:55], a[24:27]\n"
    "buffer_load_dwordx4 %[vov], %[srv], s46 offen offset:128 sc0 sc1 lds\n"
    "v_fma_f32 v110, v110, s43, v217\n"
    "v_fma_f32 v111, v111, s43, v217\n"
    "v_mfma_f32_16x16x32_bf16 a[56:59], a[208:211], v[56:59], a[56:59]\n"
    "v_exp_f32 v104, v104\n"
    "v_exp_f32 v105, v105\n"
    "v_mfma_f32_16x16x32_bf16 a[56:59], a[212:215], v[60:63], a[56:59]\n"
    "ds_read_b64_tr_b16 a[204:205], %[vb1] offset:40960\n"
    "ds_read_b64_tr_b16 a[206:207], %[vb1] offset:45056\n"
    "v_exp_f32 v106, v106\n"
    "v_mfma_f32_16x16x32_bf16 a[88:91], a[208:211], v[64:67], a[88:91]\n"
    "s_add_u32 s46, s38, 0x1100\n"
    "ds_read_b64_tr_b16 a[208:209], %[vb2] offset:32768\n"
    "ds_read_b64_tr_b16 a[210:211], %[vb2] offset:36864\n"
    "v_exp_f32 v107, v107\n"
    "v_exp_f32 v108, v108\n"
    "v_mfma_f32_16x16x32_bf16 a[88:91], a[212:215], v[68:71], a[88:91]\n"
    "s_add_u32 m0, s42, 0x4800\n"
    "v_exp_f32 v109, v109\n"
    "v_mfma_f32_16x16x32_bf16 a[28:31], a[216:219], v[48:51], a[28:31]\n"
    "v_exp_f32 v110, v110\n"
    "v_exp_f32 v111, v111\n"
    "v_mfma_f32_16x16x32_bf16 a[28:31], a[220:223], v[52:55], a[28:31]\n"
    "buffer_load_dwordx4 %[vov], %[srv], s46 offen sc0 sc1 lds\n"
    "v_cvt_pk_bf16_f32 v104, v104, v105\n"
    "v_cvt_pk_bf16_f32 v105, v106, v107\n"
    "v_cvt_pk_bf16_f32 v106, v108, v109\n"
    "v_cvt_pk_bf16_f32 v107, v110, v111\n"
    "v_mfma_f32_16x16x32_bf16 a[60:63], a[216:219], v[56:59], a[60:63]\n"
    "v_fma_f32 v112, v112, s43, v218\n"
    "v_fma_f32 v113, v113, s43, v218\n"
    "v_fma_f32 v114, v114, s43, v218\n"
    "v_mfma_f32_16x16x32_bf16 a[60:63], a[220:223], v[60:63], a[60:63]\n"
    "v_fma_f32 v115, v115, s43, v218\n"
    "v_fma_f32 v116, v116, s43, v218\n"
    "v_fma_f32 v117, v117, s43, v218\n"
    "v_mfma_f32_16x16x32_bf16 a[92:95], a[216:219], v[64:67], a[92:95]\n"
    "s_add_u32 m0, s42, 0x4b80\n"
    "v_fma_f32 v118, v118, s43, v218\n"
    "v_fma_f32 v119, v119, s43, v218\n"
    "v_mfma_f32_16x16x32_bf16 a[92:95], a[220:223], v[68:71], a[92:95]\n"
    "v_exp_f32 v112, v112\n"
    "v_exp_f32 v113, v113\n"
    "v_mfma_f32_16x16x32_bf16 a[228:231], a[224:227], v[48:51], a[228:231]\n"
    "v_exp_f32 v114, v114\n"
    "v_mfma_f32_16x16x32_bf16 a[228:231], a[224:227], v[52:55], a[228:231]\n"
    "buffer_load_dwordx4 %[vov], %[srv], s46 offen offset:128 sc0 sc1 lds\n"
    "v_exp_f32 v115, v115\n"
    "v_exp_f32 v116, v116\n"
    "v_mfma_f32_16x16x32_bf16 a[232:235], a[224:227], v[56:59], a[232:235]\n"
    "v_exp_f32 v117, v117\n"
    "v_mfma_f32_16x16x32_bf16 a[232:235], a[224:227], v[60:63], a[232:235]\n"
    "s_cbranch_vccnz .Lpad22_%=\n"
    ".Lret22_%=:\n"
    "ds_read_b64_tr_b16 a[212:213], %[vb2] offset:40960\n"
    "ds_read_b64_tr_b16 a[214:215], %[vb2] offset:45056\n"
    "v_exp_f32 v118, v118\n"
    "v_exp_f32 v119, v119\n"
    "v_mfma_f32_16x16x32_bf16 a[236:239], a[224:227], v[64:67], a[236:239]\n"
    "s_add_u32 s46, s38, 0x8100\n"
    "ds_read_b64_tr_b16 a[216:217], %[vb3] offset:32768\n"
    "ds_read_b64_tr_b16 a[218:219], %[vb3] offset:36864\n"
    "v_cvt_pk_bf16_f32 v112, v112, v113\n"
    "v_cvt_pk_bf16_f32 v113, v114, v115\n"
    "v_cvt_pk_bf16_f32 v114, v116, v117\n"
    "v_cvt_pk_bf16_f32 v115, v118, v119\n"
    "v_mfma_f32_16x16x32_bf16 a[236:239], a[224:227], v[68:71], a[236:239]\n"
    "s_add_u32 m0, s42, 0x18000\n"
    "v_fma_f32 v120, v120, s43, v216\n"
    "v_fma_f32 v121, v121, s43, v216\n"
    "v_fma_f32 v122, v122, s43, v216\n"
    "v_mfma_f32_16x16x32_bf16 a[0:3], a[192:195], v[72:75], a[0:3]\n"
    "s_waitcnt lgkmcnt(4)\n"
    "v_fma_f32 v123, v123, s43, v216\n"
    "v_fma_f32 v124, v124, s43, v216\n"
    "v_fma_f32 v125, v125, s43, v216\n"
    "v_mfma_f32_16x16x32_bf16 a[0:3], a[196:199], v[96:99], a[0:3]\n"
    "buffer_load_dwordx4 %[vov], %[srv], s46 offen sc0 sc1 lds\n"
    "v_fma_f32 v126, v126, s43, v216\n"
    "v_fma_f32 v127, v127, s43, v216\n"
    "v_mfma_f32_16x16x32_bf16 a[32:35], a[192:195], v[80:83], a[32:35]\n"
    "v_exp_f32 v120, v120\n"
    "v_exp_f32 v121, v121\n"
    "v_mfma_f32_16x16x32_bf16 a[32:35], a[196:199], v[104:107], a[32:35]\n"
    "ds_read_b64_tr_b16 a[220:221], %[vb3] offset:40960\n"
    "ds_read_b64_tr_b16 a[222:223], %[vb3] offset:45056\n"
    "v_exp_f32 v122, v122\n"
    "v_mfma_f32_16x16x32_bf16 a[64:67], a[192:195], v[88:91], a[64:67]\n"
    "s_add_u32 m0, s42, 0x18380\n"
    "ds_read_b64_tr_b16 a[192:193], %[vb0] offset:33792\n"
    "ds_read_b64_tr_b16 a[194:195], %[vb0] offset:37888\n"
    "v_exp_f32 v123, v123\n"
    "v_exp_f32 v124, v124\n"
    "v_mfma_f32_16x16x32_bf16 a[64:67], a[196:199], v[112:115], a[64:67]\n"
    "v_exp_f32 v125, v125\n"
    "v_mfma_f32_16x16x32_bf16 a[4:7], a[200:203], v[72:75], a[4:7]\n"
    "v_exp_f32 v126, v126\n"
    "v_exp_f32 v127, v127\n"
    "v_mfma_f32_16x16x32_bf16 a[4:7], a[204:207], v[96:99], a[4:7]\n"
    "buffer_load_dwordx4 %[vov], %[srv], s46 offen offset:128 sc0 sc1 lds\n"
    "v_cvt_pk_bf16_f32 v120, v120, v121\n"
    "v_cvt_pk_bf16_f32 v121, v122, v123\n"
    "v_cvt_pk_bf16_f32 v122, v124, v125\n"
    "v_cvt_pk_bf16_f32 v123, v126, v127\n"
    "v_mfma_f32_16x16x32_bf16 a[36:39], a[200:203], v[80:83], a[36:39]\n"
    "v_fma_f32 v128, v128, s43, v217\n"
    "v_fma_f32 v129, v129, s43, v217\n"
    "v_fma_f32 v130, v130, s43, v217\n"
    "v_mfma_f32_16x16x32_bf16 a[36:39], a[204:207], v[104:107], a[36:39]\n"
    "ds_read_b64_tr_b16 a[196:197], %[vb0] offset:41984\n"
    "ds_read_b64_tr_b16 a[198:199], %[vb0] offset:46080\n"
    "v_fma_f32 v131, v131, s43, v217\n"
    "v_fma_f32 v132, v132, s43, v217\n"
    "v_fma_f32 v133, v133, s43, v217\n"
    "v_mfma_f32_16x16x32_bf16 a[68:71], a[200:203], v[88:91], a[68:71]\n"
    "s_add_u32 s46, s38, 0x9100\n"
    "ds_read_b64_tr_b16 a[200:201], %[vb1] offset:33792\n"
    "ds_read_b64_tr_b16 a[202:203], %[vb1] offset:37888\n"
    "v_fma_f32 v134, v134, s43, v217\n"
    "v_fma_f32 v135, v135, s43, v217\n"
    "v_mfma_f32_16x16x32_bf16 a[68:71], a[204:207], v[112:115], a[68:71]\n"
    "s_add_u32 m0, s42, 0x18800\n"
    "v_exp_f32 v128, v128\n"
    "v_exp_f32 v129, v129\n"
    "v_mfma_f32_16x16x32_bf16 a[8:11], a[208:211], v[72:75], a[8:11]\n"
    "s_waitcnt lgkmcnt(4)\n"
    "v_exp_f32 v130, v130\n"
    "v_mfma_f32_16x16x32_bf16 a[8:11], a[212:215], v[96:99], a[8:11]\n"
    "buffer_load_dwordx4 %[vov], %[srv], s46 offen sc0 sc1 lds\n"
    "v_exp_f32 v131, v131\n"
    "v_exp_f32 v132, v132\n"
    "v_mfma_f32_16x16x32_bf16 a[40:43], a[208:211], v[80:83], a[40:43]\n"
    "v_exp_f32 v133, v133\n"
    "v_mfma_f32_16x16x32_bf16 a[40:43], a[212:215], v[104:107], a[40:43]\n"
    "ds_read_b64_tr_b16 a[204:205], %[vb1] offset:41984\n"
    "ds_read_b64_tr_b16 a[206:207], %[vb1] offset:46080\n"
    "v_exp_f32 v134, v134\n"
    "v_exp_f32 v135, v135\n"
    "v_mfma_f32_16x16x32_bf16 a[72:75], a[208:211], v[88:91], a[72:75]\n"
    "s_add_u32 m0, s42, 0x18b80\n"
    "ds_read_b64_tr_b16 a[208:209], %[vb2] offset:33792\n"
    "ds_read_b64_tr_b16 a[210:211], %[vb2] offset:37888\n"
    "v_cvt_pk_bf16_f32 v128, v128, v129\n"
    "v_cvt_pk_bf16_f32 v129, v130, v131\n"
    "v_cvt_pk_bf16_f32 v130, v132, v133\n"
    "v_cvt_pk_bf16_f32 v131, v134, v135\n"
    "v_mfma_f32_16x16x32_bf16 a[72:75], a[212:215], v[112:115], a[72:75]\n"
    "v_fma_f32 v136, v136, s43, v218\n"
    "v_fma_f32 v137, v137, s43, v218\n"
    "v_fma_f32 v138, v138, s43, v218\n"
    "v_mfma_f32_16x16x32_bf16 a[12:15], a[216:219], v[72:75], a[12:15]\n"
    "v_fma_f32 v139, v139, s43, v218\n"
    "v_fma_f32 v140, v140, s43, v218\n"
    "v_fma_f32 v141, v141, s43, v218\n"
    "v_mfma_f32_16x16x32_bf16 a[12:15], a[220:223], v[96:99], a[12:15]\n"
    "buffer_load_dwordx4 %[vov], %[srv], s46 offen offset:128 sc0 sc1 lds\n"
    "v_fma_f32 v142, v142, s43, v218\n"
    "v_fma_f32 v143, v143, s43, v218\n"
    "v_mfma_f32_16x16x32_bf16 a[44:47], a[216:219], v[80:83], a[44:47]\n"
    "v_exp_f32 v136, v136\n"
    "v_exp_f32 v137, v137\n"
    "v_mfma_f32_16x16x32_bf16 a[44:47], a[220:223], v[104:107], a[44:47]\n"
    "ds_read_b64_tr_b16 a[212:213], %[vb2] offset:41984\n"
    "ds_read_b64_tr_b16 a[214:215], %[vb2] offset:46080\n"
    "v_exp_f32 v138, v138\n"
    "v_mfma_f32_16x16x32_bf16 a[76:79], a[216:219], v[88:91], a[76:79]\n"
    "ds_read_b64_tr_b16 a[216:217], %[vb3] offset:33792\n"
    "ds_read_b64_tr_b16 a[218:219], %[vb3] offset:37888\n"
    "v_exp_f32 v139, v139\n"
    "v_exp_f32 v140, v140\n"
    "v_mfma_f32_16x16x32_bf16 a[76:79], a[220:223], v[112:115], a[76:79]\n"
    "v_exp_f32 v141, v141\n"
    "v_mfma_f32_16x16x32_bf16 a[16:19], a[192:195], v[72:75], a[16:19]\n"
    "s_waitcnt lgkmcnt(4)\n"
    "v_exp_f32 v142, v142\n"
    "v_exp_f32 v143, v143\n"
    "v_mfma_f32_16x16x32_bf16 a[16:19], a[196:199], v[96:99], a[16:19]\n"
    "v_cvt_pk_bf16_f32 v136, v136, v137\n"
    "v_cvt_pk_bf16_f32 v137, v138, v139\n"
    "v_cvt_pk_bf16_f32 v138, v140, v141\n"
    "v_cvt_pk_bf16_f32 v139, v142, v143\n"
    "v_mfma_f32_16x16x32_bf16 a[48:51], a[192:195], v[80:83], a[48:51]\n"
    "v_fma_f32 v144, v144, s43, v216\n"
    "v_fma_f32 v145, v145, s43, v216\n"
    "v_fma_f32 v146, v146, s43, v216\n"
    "v_mfma_f32_16x16x32_bf16 a[48:51], a[196:199], v[104:107], a[48:51]\n"
    "ds_read_b64_tr_b16 a[220:221], %[vb3] offset:41984\n"
    "ds_read_b64_tr_b16 a[222:223], %[vb3] offset:46080\n"
    "v_fma_f32 v147, v147, s43, v216\n"
    "v_fma_f32 v148, v148, s43, v216\n"
    "v_fma_f32 v149, v149, s43, v216\n"
    "v_mfma_f32_16x16x32_bf16 a[80:83], a[192:195], v[88:91], a[80:83]\n"
    "s_waitcnt vmcnt(20)\n"
    "s_barrier\n"
    "ds_read_b64_tr_b16 a[192:193], %[vb0] offset:0\n"
    "ds_read_b64_tr_b16 a[194:195], %[vb0] offset:4096\n"
    "v_fma_f32 v150, v150, s43, v216\n"
    "v_fma_f32 v151, v151, s43, v216\n"
    "v_mfma_f32_16x16x32_bf16 a[80:83], a[196:199], v[112:115], a[80:83]\n"
    "s_cbranch_vccnz .Lpad23_%=\n"
    ".Lret23_%=:\n"
    "v_exp_f32 v144, v144\n"
    "v_exp_f32 v145, v145\n"
    "v_mfma_f32_16x16x32_bf16 a[20:23], a[200:203], v[72:75], a[20:23]\n"
    "s_add_u32 s46, s39, 0x5000\n"
    "v_exp_f32 v146, v146\n"
    "v_mfma_f32_16x16x32_bf16 a[20:23], a[204:207], v[96:99], a[20:23]\n"
    "s_add_u32 m0, s42, 0xc000\n"
    "v_exp_f32 v147, v147\n"
    "v_exp_f32 v148, v148\n"
    "v_mfma_f32_16x16x32_bf16 a[52:55], a[200:203], v[80:83], a[52:55]\n"
    "buffer_load_dwordx4 %[voo], %[srq], s46 offen nt lds\n"
    "v_exp_f32 v149, v149\n"
    "v_mfma_f32_16x16x32_bf16 a[52:55], a[204:207], v[104:107], a[52:55]\n"
    "ds_read_b64_tr_b16 a[196:197], %[vb0] offset:8192\n"
    "ds_read_b64_tr_b16 a[198:199], %[vb0] offset:12288\n"
    "v_exp_f32 v150, v150\n"
    "v_exp_f32 v151, v151\n"
    "v_mfma_f32_16x16x32_bf16 a[84:87], a[200:203], v[88:91], a[84:87]\n"
    "s_add_u32 m0, s42, 0xc380\n"
    "ds_read_b64_tr_b16 a[200:201], %[vb1] offset:0\n"
    "ds_read_b64_tr_b16 a[202:203], %[vb1] offset:4096\n"
    "v_cvt_pk_bf16_f32 v144, v144, v145\n"
    "v_cvt_pk_bf16_f32 v145, v146, v147\n"
    "v_cvt_pk_bf16_f32 v146, v148, v149\n"
    "v_cvt_pk_bf16_f32 v147, v150, v151\n"
    "v_mfma_f32_16x16x32_bf16 a[84:87], a[204:207], v[112:115], a[84:87]\n"
    "v_fma_f32 v152, v152, s43, v217\n"
    "v_fma_f32 v153, v153, s43, v217\n"
    "v_fma_f32 v154, v154, s43, v217\n"
    "v_mfma_f32_16x16x32_bf16 a[24:27], a[208:211], v[72:75], a[24:27]\n"
    "s_waitcnt lgkmcnt(4)\n"
    "v_fma_f32 v155, v155, s43, v217\n"
    "v_fma_f32 v156, v156, s43, v217\n"
    "v_fma_f32 v157, v157, s43, v217\n"
    "v_mfma_f32_16x16x32_bf16 a[24:27], a[212:215], v[96:99], a[24:27]\n"
    "s_waitcnt vmcnt(13)\n"
    "buffer_load_dwordx4 %[voo], %[srq], s46 offen offset:128 nt lds\n"
    "v_fma_f32 v158, v158, s43, v217\n"
    "v_fma_f32 v159, v159, s43, v217\n"
    "v_mfma_f32_16x16x32_bf16 a[56:59], a[208:211], v[80:83], a[56:59]\n"
    "ds_read_b128 a[128:131], %[qb0] offset:0\n"
    "v_exp_f32 v152, v152\n"
    "v_exp_f32 v153, v153\n"
    "v_mfma_f32_16x16x32_bf16 a[56:59], a[212:215], v[104:107], a[56:59]\n"
    "ds_read_b64_tr_b16 a[204:205], %[vb1] offset:8192\n"
    "ds_read_b64_tr_b16 a[206:207], %[vb1] offset:12288\n"
    "v_exp_f32 v154, v154\n"
    "v_mfma_f32_16x16x32_bf16 a[88:91], a[208:211], v[88:91], a[88:91]\n"
    "s_add_u32 m0, s42, 0xc700\n"
    "ds_read_b64_tr_b16 a[208:209], %[vb2] offset:0\n"
    "ds_read_b64_tr_b16 a[210:211], %[vb2] offset:4096\n"
    "v_exp_f32 v155, v155\n"
    "v_exp_f32 v156, v156\n"
    "v_mfma_f32_16x16x32_bf16 a[88:91], a[212:215], v[112:115], a[88:91]\n"
    "ds_read_b128 a[132:135], %[qb1] offset:0\n"
    "v_exp_f32 v157, v157\n"
    "v_mfma_f32_16x16x32_bf16 a[28:31], a[216:219], v[72:75], a[28:31]\n"
    "ds_read_b128 a[136:139], %[qb0] offset:1024\n"
    "v_exp_f32 v158, v158\n"
    "v_exp_f32 v159, v159\n"
    "v_mfma_f32_16x16x32_bf16 a[28:31], a[220:223], v[96:99], a[28:31]\n"
    "buffer_load_dwordx4 %[voo], %[srq], s46 offen offset:256 nt lds\n"
    "v_cvt_pk_bf16_f32 v152, v152, v153\n"
    "v_cvt_pk_bf16_f32 v153, v154, v155\n"
    "v_cvt_pk_bf16_f32 v154, v156, v157\n"
    "v_cvt_pk_bf16_f32 v155, v158, v159\n"
    "v_mfma_f32_16x16x32_bf16 a[60:63], a[216:219], v[80:83], a[60:63]\n"
    "ds_read_b128 a[140:143], %[qb1] offset:1024\n"
    "v_fma_f32 v160, v160, s43, v218\n"
    "v_fma_f32 v161, v161, s43, v218\n"
    "v_fma_f32 v162, v162, s43, v218\n"
    "v_mfma_f32_16x16x32_bf16 a[60:63], a[220:223], v[104:107], a[60:63]\n"
    "ds_read_b128 a[144:147], %[qb0] offset:2048\n"
    "v_fma_f32 v163, v163, s43, v218\n"
    "v_fma_f32 v164, v164, s43, v218\n"
    "v_fma_f32 v165, v165, s43, v218\n"
    "v_mfma_f32_16x16x32_bf16 a[92:95], a[216:219], v[88:91], a[92:95]\n"
    "s_add_u32 m0, s42, 0xca80\n"
    "ds_read_b128 a[148:151], %[qb1] offset:2048\n"
    "v_fma_f32 v166, v166, s43, v218\n"
    "v_fma_f32 v167, v167, s43, v218\n"
    "v_mfma_f32_16x16x32_bf16 a[92:95], a[220:223], v[112:115], a[92:95]\n"
    "ds_read_b128 a[152:155], %[qb0] offset:3072\n"
    "v_exp_f32 v160, v160\n"
    "v_exp_f32 v161, v161\n"
    "v_mfma_f32_16x16x32_bf16 a[228:231], a[224:227], v[72:75], a[228:231]\n"
    "s_waitcnt lgkmcnt(8)\n"
    "ds_read_b128 a[156:159], %[qb1] offset:3072\n"
    "v_exp_f32 v162, v162\n"
    "v_mfma_f32_16x16x32_bf16 a[228:231], a[224:227], v[96:99], a[228:231]\n"
    "buffer_load_dwordx4 %[voo], %[srq], s46 offen offset:384 nt lds\n"
    "v_exp_f32 v163, v163\n"
    "v_exp_f32 v164, v164\n"
    "v_mfma_f32_16x16x32_bf16 a[232:235], a[224:227], v[80:83], a[232:235]\n"
    "v_exp_f32 v165, v165\n"
    "v_mfma_f32_16x16x32_bf16 a[232:235], a[224:227], v[104:107], a[232:235]\n"
    "ds_read_b64_tr_b16 a[212:213], %[vb2] offset:8192\n"
    "ds_read_b64_tr_b16 a[214:215], %[vb2] offset:12288\n"
    "v_exp_f32 v166, v166\n"
    "v_exp_f32 v167, v167\n"
    "v_mfma_f32_16x16x32_bf16 a[236:239], a[224:227], v[88:91], a[236:239]\n"
    "ds_read_b64_tr_b16 a[216:217], %[vb3] offset:0\n"
    "ds_read_b64_tr_b16 a[218:219], %[vb3] offset:4096\n"
    "v_cvt_pk_bf16_f32 v160, v160, v161\n"
    "v_cvt_pk_bf16_f32 v161, v162, v163\n"
    "v_cvt_pk_bf16_f32 v162, v164, v165\n"
    "v_cvt_pk_bf16_f32 v163, v166, v167\n"
    "v_mfma_f32_16x16x32_bf16 a[236:239], a[224:227], v[112:115], a[236:239]\n"
    "v_fma_f32 v168, v168, s43, v216\n"
    "v_fma_f32 v169, v169, s43, v216\n"
    "v_mfma_f32_16x16x32_bf16 a[0:3], a[192:195], v[120:123], a[0:3]\n"
    "v_fma_f32 v170, v170, s43, v216\n"
    "v_fma_f32 v171, v171, s43, v216\n"
    "v_mfma_f32_16x16x32_bf16 a[0:3], a[196:199], v[144:147], a[0:3]\n"
    "v_fma_f32 v172, v172, s43, v216\n"
    "v_fma_f32 v173, v173, s43, v216\n"
    "v_mfma_f32_16x16x32_bf16 a[32:35], a[192:195], v[128:131], a[32:35]\n"
    "v_fma_f32 v174, v174, s43, v216\n"
    "v_fma_f32 v175, v175, s43, v216\n"
    "v_mfma_f32_16x16x32_bf16 a[32:35], a[196:199], v[152:155], a[32:35]\n"
    "ds_read_b64_tr_b16 a[220:221], %[vb3] offset:8192\n"
    "ds_read_b64_tr_b16 a[222:223], %[vb3] offset:12288\n"
    "v_mfma_f32_16x16x32_bf16 a[64:67], a[192:195], v[136:139], a[64:67]\n"
    "s_waitcnt lgkmcnt(8)\n"
    "ds_read_b64_tr_b16 a[192:193], %[vb0] offset:1024\n"
    "v_mfma_f32_16x16x32_bf16 a[64:67], a[196:199], v[160:163], a[64:67]\n"
    "ds_read_b64_tr_b16 a[194:195], %[vb0] offset:5120\n"
    "v_exp_f32 v168, v168\n"
    "v_mfma_f32_16x16x32_bf16 a[4:7], a[200:203], v[120:123], a[4:7]\n"
    "v_exp_f32 v169, v169\n"
    "v_mfma_f32_16x16x32_bf16 a[4:7], a[204:207], v[144:147], a[4:7]\n"
    "s_cbranch_vccnz .Lpad24_%=\n"
    ".Lret24_%=:\n"
    "v_exp_f32 v170, v170\n"
    "v_mfma_f32_16x16x32_bf16 a[36:39], a[200:203], v[128:131], a[36:39]\n"
    "s_add_u32 s46, s38, 0x10100\n"
    "v_exp_f32 v171, v171\n"
    "v_mfma_f32_16x16x32_bf16 a[36:39], a[204:207], v[152:155], a[36:39]\n"
    "s_add_u32 m0, s42, 0x10000\n"
    "ds_read_b64_tr_b16 a[196:197], %[vb0] offset:9216\n"
    "ds_read_b64_tr_b16 a[198:199], %[vb0] offset:13312\n"
    "v_mfma_f32_16x16x32_bf16 a[68:71], a[200:203], v[136:139], a[68:71]\n"
    "s_waitcnt lgkmcnt(6)\n"
    "buffer_load_dwordx4 %[vov], %[srv], s46 offen sc0 sc1 lds\n"
    "v_exp_f32 v172, v172\n"
    "v_mfma_f32_16x16x32_bf16 a[68:71], a[204:207], v[160:163], a[68:71]\n"
    "ds_read_b64_tr_b16 a[200:201], %[vb1] offset:1024\n"
    "ds_read_b64_tr_b16 a[202:203], %[vb1] offset:5120\n"
    "v_exp_f32 v173, v173\n"
    "v_mfma_f32_16x16x32_bf16 a[8:11], a[208:211], v[120:123], a[8:11]\n"
    "s_add_u32 m0, s42, 0x10380\n"
    "v_exp_f32 v174, v174\n"
    "v_mfma_f32_16x16x32_bf16 a[8:11], a[212:215], v[144:147], a[8:11]\n"
    "v_exp_f32 v175, v175\n"
    "v_mfma_f32_16x16x32_bf16 a[40:43], a[208:211], v[128:131], a[40:43]\n"
    "v_cvt_pk_bf16_f32 v168, v168, v169\n"
    "v_cvt_pk_bf16_f32 v169, v170, v171\n"
    "v_mfma_f32_16x16x32_bf16 a[40:43], a[212:215], v[152:155], a[40:43]\n"
    "ds_read_b64_tr_b16 a[204:205], %[vb1] offset:9216\n"
    "ds_read_b64_tr_b16 a[206:207], %[vb1] offset:13312\n"
    "v_mfma_f32_16x16x32_bf16 a[72:75], a[208:211], v[136:139], a[72:75]\n"
    "buffer_load_dwordx4 %[vov], %[srv], s46 offen offset:128 sc0 sc1 lds\n"
    "v_cvt_pk_bf16_f32 v170, v172, v173\n"
    "v_cvt_pk_bf16_f32 v171, v174, v175\n"
    "v_mfma_f32_16x16x32_bf16 a[72:75], a[212:215], v[160:163], a[72:75]\n"
    "ds_read_b64_tr_b16 a[208:209], %[vb2] offset:1024\n"
    "ds_read_b64_tr_b16 a[210:211], %[vb2] offset:5120\n"
    "v_fma_f32 v176, v176, s43, v217\n"
    "v_mfma_f32_16x16x32_bf16 a[12:15], a[216:219], v[120:123], a[12:15]\n"
    "s_waitcnt lgkmcnt(6)\n"
    "v_fma_f32 v177, v177, s43, v217\n"
    "v_fma_f32 v178, v178, s43, v217\n"
    "v_mfma_f32_16x16x32_bf16 a[12:15], a[220:223], v[144:147], a[12:15]\n"
    "s_add_u32 s46, s38, 0x11100\n"
    "v_fma_f32 v179, v179, s43, v217\n"
    "v_fma_f32 v180, v180, s43, v217\n"
    "v_mfma_f32_16x16x32_bf16 a[44:47], a[216:219], v[128:131], a[44:47]\n"
    "s_add_u32 m0, s42, 0x10800\n"
    "v_fma_f32 v181, v181, s43, v217\n"
    "v_fma_f32 v182, v182, s43, v217\n"
    "v_mfma_f32_16x16x32_bf16 a[44:47], a[220:223], v[152:155], a[44:47]\n"
    "ds_read_b64_tr_b16 a[212:213], %[vb2] offset:9216\n"
    "ds_read_b64_tr_b16 a[214:215], %[vb2] offset:13312\n"
    "v_mfma_f32_16x16x32_bf16 a[76:79], a[216:219], v[136:139], a[76:79]\n"
    "buffer_load_dwordx4 %[vov], %[srv], s46 offen sc0 sc1 lds\n"
    "v_fma_f32 v183, v183, s43, v217\n"
    "v_mfma_f32_16x16x32_bf16 a[76:79], a[220:223], v[160:163], a[76:79]\n"
    "ds_read_b64_tr_b16 a[216:217], %[vb3] offset:1024\n"
    "ds_read_b64_tr_b16 a[218:219], %[vb3] offset:5120\n"
    "v_exp_f32 v176, v176\n"
    "v_mfma_f32_16x16x32_bf16 a[16:19], a[192:195], v[120:123], a[16:19]\n"
    "s_add_u32 m0, s42, 0x10b80\n"
    "v_exp_f32 v177, v177\n"
    "v_mfma_f32_16x16x32_bf16 a[16:19], a[196:199], v[144:147], a[16:19]\n"
    "v_exp_f32 v178, v178\n"
    "v_mfma_f32_16x16x32_bf16 a[48:51], a[192:195], v[128:131], a[48:51]\n"
    "v_exp_f32 v179, v179\n"
    "v_mfma_f32_16x16x32_bf16 a[48:51], a[196:199], v[152:155], a[48:51]\n"
    "ds_read_b64_tr_b16 a[220:221], %[vb3] offset:9216\n"
    "ds_read_b64_tr_b16 a[222:223], %[vb3] offset:13312\n"
    "v_mfma_f32_16x16x32_bf16 a[80:83], a[192:195], v[136:139], a[80:83]\n"
    "s_waitcnt vmcnt(15)\n"
    "s_barrier\n"
    "buffer_load_dwordx4 %[vov], %[srv], s46 offen offset:128 sc0 sc1 lds\n"
    "v_exp_f32 v180, v180\n"
    "v_mfma_f32_16x16x32_bf16 a[80:83], a[196:199], v[160:163], a[80:83]\n"
    "s_waitcnt lgkmcnt(4)\n"
    "v_exp_f32 v181, v181\n"
    "v_mfma_f32_16x16x32_bf16 a[20:23], a[200:203], v[120:123], a[20:23]\n"
    "s_add_u32 s46, s39, 0x4000\n"
    "v_add_u32 %[vb0], 0x10000, %[vb0]\n"
    "v_add_u32 %[vb1], 0x10000, %[vb1]\n"
    "v_add_u32 %[vb2], 0x10000, %[vb2]\n"
    "v_add_u32 %[vb3], 0x10000, %[vb3]\n"
    "ds_read_b64_tr_b16 a[192:193], %[vb0] offset:49152\n"
    "ds_read_b64_tr_b16 a[194:195], %[vb0] offset:53248\n"
    "v_mfma_f32_16x16x32_bf16 a[20:23], a[204:207], v[144:147], a[20:23]\n"
    "s_add_u32 m0, s42, 0x8000\n"
    "v_exp_f32 v182, v182\n"
    "v_mfma_f32_16x16x32_bf16 a[52:55], a[200:203], v[128:131], a[52:55]\n"
    "v_exp_f32 v183, v183\n"
    "v_mfma_f32_16x16x32_bf16 a[52:55], a[204:207], v[152:155], a[52:55]\n"
    "ds_read_b64_tr_b16 a[196:197], %[vb1] offset:49152\n"
    "ds_read_b64_tr_b16 a[198:199], %[vb1] offset:53248\n"
    "v_cvt_pk_bf16_f32 v176, v176, v177\n"
    "v_cvt_pk_bf16_f32 v177, v178, v179\n"
    "v_mfma_f32_16x16x32_bf16 a[84:87], a[200:203], v[136:139], a[84:87]\n"
    "buffer_load_dwordx4 %[voe], %[srq], s46 offen nt lds\n"
    "v_cvt_pk_bf16_f32 v178, v180, v181\n"
    "v_cvt_pk_bf16_f32 v179, v182, v183\n"
    "v_mfma_f32_16x16x32_bf16 a[84:87], a[204:207], v[160:163], a[84:87]\n"
    "ds_read_b64_tr_b16 a[200:201], %[vb2] offset:49152\n"
    "ds_read_b64_tr_b16 a[202:203], %[vb2] offset:53248\n"
    "v_fma_f32 v184, v184, s43, v218\n"
    "v_mfma_f32_16x16x32_bf16 a[24:27], a[208:211], v[120:123], a[24:27]\n"
    "s_add_u32 m0, s42, 0x8380\n"
    "v_fma_f32 v185, v185, s43, v218\n"
    "v_fma_f32 v186, v186, s43, v218\n"
    "v_mfma_f32_16x16x32_bf16 a[24:27], a[212:215], v[144:147], a[24:27]\n"
    "v_fma_f32 v187, v187, s43, v218\n"
    "v_fma_f32 v188, v188, s43, v218\n"
    "v_mfma_f32_16x16x32_bf16 a[56:59], a[208:211], v[128:131], a[56:59]\n"
    "v_fma_f32 v189, v189, s43, v218\n"
    "v_fma_f32 v190, v190, s43, v218\n"
    "v_mfma_f32_16x16x32_bf16 a[56:59], a[212:215], v[152:155], a[56:59]\n"
    "s_cbranch_vccnz .Lpad25_%=\n"
    ".Lret25_%=:\n"
    "ds_read_b64_tr_b16 a[204:205], %[vb3] offset:49152\n"
    "ds_read_b64_tr_b16 a[206:207], %[vb3] offset:53248\n"
    "v_mfma_f32_16x16x32_bf16 a[88:91], a[208:211], v[136:139], a[88:91]\n"
    "buffer_load_dwordx4 %[voe], %[srq], s46 offen offset:128 nt lds\n"
    "v_fma_f32 v191, v191, s43, v218\n"
    "v_mfma_f32_16x16x32_bf16 a[88:91], a[212:215], v[160:163], a[88:91]\n"
    "s_waitcnt lgkmcnt(5)\n"
    "ds_read_b64_tr_b16 a[208:209], %[vb0] offset:50176\n"
    "ds_read_b64_tr_b16 a[210:211], %[vb0] offset:54272\n"
    "v_exp_f32 v184, v184\n"
    "v_mfma_f32_16x16x32_bf16 a[28:31], a[216:219], v[120:123], a[28:31]\n"
    "s_add_u32 m0, s42, 0x8700\n"
    "v_exp_f32 v185, v185\n"
    "v_mfma_f32_16x16x32_bf16 a[28:31], a[220:223], v[144:147], a[28:31]\n"
    "v_exp_f32 v186, v186\n"
    "v_mfma_f32_16x16x32_bf16 a[60:63], a[216:219], v[128:131], a[60:63]\n"
    "v_exp_f32 v187, v187\n"
    "v_mfma_f32_16x16x32_bf16 a[60:63], a[220:223], v[152:155], a[60:63]\n"
    "buffer_load_dwordx4 %[voe], %[srq], s46 offen offset:256 nt lds\n"
    "v_mfma_f32_16x16x32_bf16 a[92:95], a[216:219], v[136:139], a[92:95]\n"
    "v_exp_f32 v188, v188\n"
    "v_mfma_f32_16x16x32_bf16 a[92:95], a[220:223], v[160:163], a[92:95]\n"
    "v_exp_f32 v189, v189\n"
    "v_mfma_f32_16x16x32_bf16 a[228:231], a[224:227], v[120:123], a[228:231]\n"
    "s_add_u32 m0, s42, 0x8a80\n"
    "v_exp_f32 v190, v190\n"
    "v_mfma_f32_16x16x32_bf16 a[228:231], a[224:227], v[144:147], a[228:231]\n"
    "v_exp_f32 v191, v191\n"
    "v_mfma_f32_16x16x32_bf16 a[232:235], a[224:227], v[128:131], a[232:235]\n"
    "v_cvt_pk_bf16_f32 v184, v184, v185\n"
    "v_cvt_pk_bf16_f32 v185, v186, v187\n"
    "v_mfma_f32_16x16x32_bf16 a[232:235], a[224:227], v[152:155], a[232:235]\n"
    "ds_read_b64_tr_b16 a[212:213], %[vb1] offset:50176\n"
    "ds_read_b64_tr_b16 a[214:215], %[vb1] offset:54272\n"
    "v_mfma_f32_16x16x32_bf16 a[236:239], a[224:227], v[136:139], a[236:239]\n"
    "buffer_load_dwordx4 %[voe], %[srq], s46 offen offset:384 nt lds\n"
    "v_cvt_pk_bf16_f32 v186, v188, v189\n"
    "v_cvt_pk_bf16_f32 v187, v190, v191\n"
    "v_mfma_f32_16x16x32_bf16 a[236:239], a[224:227], v[160:163], a[236:239]\n"
    "v_fma_f32 v192, v192, s43, v216\n"
    "v_fma_f32 v193, v193, s43, v216\n"
    "v_fma_f32 v194, v194, s43, v216\n"
    "v_mfma_f32_16x16x32_bf16 a[0:3], a[192:195], v[168:171], a[0:3]\n"
    "s_add_u32 s46, s38, 0x18100\n"
    "ds_read_b64_tr_b16 a[216:217], %[vb2] offset:50176\n"
    "ds_read_b64_tr_b16 a[218:219], %[vb2] offset:54272\n"
    "v_fma_f32 v195, v195, s43, v216\n"
    "v_fma_f32 v196, v196, s43, v216\n"
    "v_fma_f32 v197, v197, s43, v216\n"
    "v_mfma_f32_16x16x32_bf16 a[32:35], a[192:195], v[176:179], a[32:35]\n"
    "s_add_u32 m0, s42, 0x14000\n"
    "v_fma_f32 v198, v198, s43, v216\n"
    "v_fma_f32 v199, v199, s43, v216\n"
    "v_mfma_f32_16x16x32_bf16 a[64:67], a[192:195], v[184:187], a[64:67]\n"
    "s_waitcnt lgkmcnt(4)\n"
    "v_exp_f32 v192, v192\n"
    "v_exp_f32 v193, v193\n"
    "v_mfma_f32_16x16x32_bf16 a[4:7], a[196:199], v[168:171], a[4:7]\n"
    "ds_read_b64_tr_b16 a[220:221], %[vb3] offset:50176\n"
    "ds_read_b64_tr_b16 a[222:223], %[vb3] offset:54272\n"
    "v_exp_f32 v194, v194\n"
    "v_mfma_f32_16x16x32_bf16 a[36:39], a[196:199], v[176:179], a[36:39]\n"
    "buffer_load_dwordx4 %[vov], %[srv], s46 offen sc0 sc1 lds\n"
    "v_exp_f32 v195, v195\n"
    "v_exp_f32 v196, v196\n"
    "v_mfma_f32_16x16x32_bf16 a[68:71], a[196:199], v[184:187], a[68:71]\n"
    "v_exp_f32 v197, v197\n"
    "v_mfma_f32_16x16x32_bf16 a[8:11], a[200:203], v[168:171], a[8:11]\n"
    "s_add_u32 m0, s42, 0x14380\n"
    "ds_read_b64_tr_b16 a[192:193], %[vb0] offset:57344\n"
    "ds_read_b64_tr_b16 a[194:195], %[vb0] offset:61440\n"
    "v_exp_f32 v198, v198\n"
    "v_exp_f32 v199, v199\n"
    "v_mfma_f32_16x16x32_bf16 a[40:43], a[200:203], v[176:179], a[40:43]\n"
    "v_cvt_pk_bf16_f32 v192, v192, v193\n"
    "v_cvt_pk_bf16_f32 v193, v194, v195\n"
    "v_cvt_pk_bf16_f32 v194, v196, v197\n"
    "v_cvt_pk_bf16_f32 v195, v198, v199\n"
    "v_mfma_f32_16x16x32_bf16 a[72:75], a[200:203], v[184:187], a[72:75]\n"
    "v_fma_f32 v200, v200, s43, v217\n"
    "v_fma_f32 v201, v201, s43, v217\n"
    "v_fma_f32 v202, v202, s43, v217\n"
    "v_mfma_f32_16x16x32_bf16 a[12:15], a[204:207], v[168:171], a[12:15]\n"
    "ds_read_b64_tr_b16 a[196:197], %[vb1] offset:57344\n"
    "ds_read_b64_tr_b16 a[198:199], %[vb1] offset:61440\n"
    "v_fma_f32 v203, v203, s43, v217\n"
    "v_fma_f32 v204, v204, s43, v217\n"
    "v_fma_f32 v205, v205, s43, v217\n"
    "v_mfma_f32_16x16x32_bf16 a[44:47], a[204:207], v[176:179], a[44:47]\n"
    "buffer_load_dwordx4 %[vov], %[srv], s46 offen offset:128 sc0 sc1 lds\n"
    "v_fma_f32 v206, v206, s43, v217\n"
    "v_fma_f32 v207, v207, s43, v217\n"
    "v_mfma_f32_16x16x32_bf16 a[76:79], a[204:207], v[184:187], a[76:79]\n"
    "v_exp_f32 v200, v200\n"
    "v_exp_f32 v201, v201\n"
    "v_mfma_f32_16x16x32_bf16 a[16:19], a[208:211], v[168:171], a[16:19]\n"
    "s_add_u32 s46, s38, 0x19100\n"
    "ds_read_b64_tr_b16 a[200:201], %[vb2] offset:57344\n"
    "ds_read_b64_tr_b16 a[202:203], %[vb2] offset:61440\n"
    "v_exp_f32 v202, v202\n"
    "v_mfma_f32_16x16x32_bf16 a[48:51], a[208:211], v[176:179], a[48:51]\n"
    "s_add_u32 m0, s42, 0x14800\n"
    "v_exp_f32 v203, v203\n"
    "v_exp_f32 v204, v204\n"
    "v_mfma_f32_16x16x32_bf16 a[80:83], a[208:211], v[184:187], a[80:83]\n"
    "s_waitcnt lgkmcnt(4)\n"
    "v_exp_f32 v205, v205\n"
    "v_mfma_f32_16x16x32_bf16 a[20:23], a[212:215], v[168:171], a[20:23]\n"
    "ds_read_b64_tr_b16 a[204:205], %[vb3] offset:57344\n"
    "ds_read_b64_tr_b16 a[206:207], %[vb3] offset:61440\n"
    "v_exp_f32 v206, v206\n"
    "v_exp_f32 v207, v207\n"
    "v_mfma_f32_16x16x32_bf16 a[52:55], a[212:215], v[176:179], a[52:55]\n"
    "buffer_load_dwordx4 %[vov], %[srv], s46 offen sc0 sc1 lds\n"
    "v_cvt_pk_bf16_f32 v200, v200, v201\n"
    "v_cvt_pk_bf16_f32 v201, v202, v203\n"
    "v_cvt_pk_bf16_f32 v202, v204, v205\n"
    "v_cvt_pk_bf16_f32 v203, v206, v207\n"
    "v_mfma_f32_16x16x32_bf16 a[84:87], a[212:215], v[184:187], a[84:87]\n"
    "s_cbranch_vccnz .Lpad26_%=\n"
    ".Lret26_%=:\n"
    "v_fma_f32 v208, v208, s43, v218\n"
    "v_fma_f32 v209, v209, s43, v218\n"
    "v_fma_f32 v210, v210, s43, v218\n"
    "v_mfma_f32_16x16x32_bf16 a[24:27], a[216:219], v[168:171], a[24:27]\n"
    "s_add_u32 m0, s42, 0x14b80\n"
    "ds_read_b64_tr_b16 a[208:209], %[vb0] offset:58368\n"
    "ds_read_b64_tr_b16 a[210:211], %[vb0] offset:62464\n"
    "v_fma_f32 v211, v211, s43, v218\n"
    "v_fma_f32 v212, v212, s43, v218\n"
    "v_fma_f32 v213, v213, s43, v218\n"
    "v_mfma_f32_16x16x32_bf16 a[56:59], a[216:219], v[176:179], a[56:59]\n"
    "v_fma_f32 v214, v214, s43, v218\n"
    "v_fma_f32 v215, v215, s43, v218\n"
    "v_mfma_f32_16x16x32_bf16 a[88:91], a[216:219], v[184:187], a[88:91]\n"
    "v_exp_f32 v208, v208\n"
    "v_exp_f32 v209, v209\n"
    "v_mfma_f32_16x16x32_bf16 a[28:31], a[220:223], v[168:171], a[28:31]\n"
    "buffer_load_dwordx4 %[vov], %[srv], s46 offen offset:128 sc0 sc1 lds\n"
    "v_exp_f32 v210, v210\n"
    "v_mfma_f32_16x16x32_bf16 a[60:63], a[220:223], v[176:179], a[60:63]\n"
    "v_exp_f32 v211, v211\n"
    "v_exp_f32 v212, v212\n"
    "v_mfma_f32_16x16x32_bf16 a[92:95], a[220:223], v[184:187], a[92:95]\n"
    "v_exp_f32 v213, v213\n"
    "v_mfma_f32_16x16x32_bf16 a[228:231], a[224:227], v[168:171], a[228:231]\n"
    "ds_read_b64_tr_b16 a[212:213], %[vb1] offset:58368\n"
    "ds_read_b64_tr_b16 a[214:215], %[vb1] offset:62464\n"
    "v_exp_f32 v214, v214\n"
    "v_exp_f32 v215, v215\n"
    "v_mfma_f32_16x16x32_bf16 a[232:235], a[224:227], v[176:179], a[232:235]\n"
    "v_cvt_pk_bf16_f32 v208, v208, v209\n"
    "v_cvt_pk_bf16_f32 v209, v210, v211\n"
    "v_cvt_pk_bf16_f32 v210, v212, v213\n"
    "v_cvt_pk_bf16_f32 v211, v214, v215\n"
    "v_mfma_f32_16x16x32_bf16 a[236:239], a[224:227], v[184:187], a[236:239]\n"
    "v_mfma_f32_16x16x32_bf16 a[0:3], a[192:195], v[192:195], a[0:3]\n"
    "ds_read_b64_tr_b16 a[216:217], %[vb2] offset:58368\n"
    "ds_read_b64_tr_b16 a[218:219], %[vb2] offset:62464\n"
    "v_mfma_f32_16x16x32_bf16 a[32:35], a[192:195], v[200:203], a[32:35]\n"
    "v_mfma_f32_16x16x32_bf16 a[64:67], a[192:195], v[208:211], a[64:67]\n"
    "s_waitcnt lgkmcnt(4)\n"
    "v_mfma_f32_16x16x32_bf16 a[4:7], a[196:199], v[192:195], a[4:7]\n"
    "ds_read_b64_tr_b16 a[220:221], %[vb3] offset:58368\n"
    "ds_read_b64_tr_b16 a[222:223], %[vb3] offset:62464\n"
    "v_mfma_f32_16x16x32_bf16 a[36:39], a[196:199], v[200:203], a[36:39]\n"
    "v_mfma_f32_16x16x32_bf16 a[68:71], a[196:199], v[208:211], a[68:71]\n"
    "v_mfma_f32_16x16x32_bf16 a[8:11], a[200:203], v[192:195], a[8:11]\n"
    "s_waitcnt vmcnt(20)\n"
    "s_barrier\n"
    "v_add_u32 %[vb0], 0xffff0000, %[vb0]\n"
    "v_add_u32 %[vb1], 0xffff0000, %[vb1]\n"
    "v_mfma_f32_16x16x32_bf16 a[40:43], a[200:203], v[200:203], a[40:43]\n"
    "v_add_u32 %[vb2], 0xffff0000, %[vb2]\n"
    "v_add_u32 %[vb3], 0xffff0000, %[vb3]\n"
    "v_mfma_f32_16x16x32_bf16 a[72:75], a[200:203], v[208:211], a[72:75]\n"
    "s_add_u32 s46, s38, 0x20100\n"
    "ds_read_b64_tr_b16 a[192:193], %[vb0] offset:16384\n"
    "ds_read_b64_tr_b16 a[194:195], %[vb0] offset:20480\n"
    "v_mfma_f32_16x16x32_bf16 a[12:15], a[204:207], v[192:195], a[12:15]\n"
    "s_add_u32 m0, s42, 0x0\n"
    "ds_read_b64_tr_b16 a[196:197], %[vb0] offset:24576\n"
    "ds_read_b64_tr_b16 a[198:199], %[vb0] offset:28672\n"
    "v_mfma_f32_16x16x32_bf16 a[44:47], a[204:207], v[200:203], a[44:47]\n"
    "buffer_load_dwordx4 %[vov], %[srv], s46 offen sc0 sc1 lds\n"
    "v_mfma_f32_16x16x32_bf16 a[76:79], a[204:207], v[208:211], a[76:79]\n"
    "v_mfma_f32_16x16x32_bf16 a[16:19], a[208:211], v[192:195], a[16:19]\n"
    "s_add_u32 m0, s42, 0x380\n"
    "ds_read_b64_tr_b16 a[200:201], %[vb1] offset:16384\n"
    "ds_read_b64_tr_b16 a[202:203], %[vb1] offset:20480\n"
    "v_mfma_f32_16x16x32_bf16 a[48:51], a[208:211], v[200:203], a[48:51]\n"
    "v_mfma_f32_16x16x32_bf16 a[80:83], a[208:211], v[208:211], a[80:83]\n"
    "s_waitcnt lgkmcnt(6)\n"
    "v_mfma_f32_16x16x32_bf16 a[20:23], a[212:215], v[192:195], a[20:23]\n"
    "ds_read_b64_tr_b16 a[204:205], %[vb1] offset:24576\n"
    "ds_read_b64_tr_b16 a[206:207], %[vb1] offset:28672\n"
    "v_mfma_f32_16x16x32_bf16 a[52:55], a[212:215], v[200:203], a[52:55]\n"
    "buffer_load_dwordx4 %[vov], %[srv], s46 offen offset:128 sc0 sc1 lds\n"
    "v_mfma_f32_16x16x32_bf16 a[84:87], a[212:215], v[208:211], a[84:87]\n"
    "v_mfma_f32_16x16x32_bf16 a[24:27], a[216:219], v[192:195], a[24:27]\n"
    "s_add_u32 s46, s38, 0x21100\n"
    "ds_read_b64_tr_b16 a[208:209], %[vb2] offset:16384\n"
    "ds_read_b64_tr_b16 a[210:211], %[vb2] offset:20480\n"
    "v_mfma_f32_16x16x32_bf16 a[56:59], a[216:219], v[200:203], a[56:59]\n"
    "s_add_u32 m0, s42, 0x800\n"
    "v_mfma_f32_16x16x32_bf16 a[88:91], a[216:219], v[208:211], a[88:91]\n"
    "v_mfma_f32_16x16x32_bf16 a[28:31], a[220:223], v[192:195], a[28:31]\n"
    "buffer_load_dwordx4 %[vov], %[srv], s46 offen sc0 sc1 lds\n"
    "v_mfma_f32_16x16x32_bf16 a[60:63], a[220:223], v[200:203], a[60:63]\n"
    "s_cbranch_vccnz .Lpad27_%=\n"
    ".Lret27_%=:\n"
    "v_mfma_f32_16x16x32_bf16 a[92:95], a[220:223], v[208:211], a[92:95]\n"
    "v_mfma_f32_16x16x32_bf16 a[228:231], a[224:227], v[192:195], a[228:231]\n"
    "s_add_u32 m0, s42, 0xb80\n"
    "ds_read_b64_tr_b16 a[212:213], %[vb2] offset:24576\n"
    "ds_read_b64_tr_b16 a[214:215], %[vb2] offset:28672\n"
    "v_mfma_f32_16x16x32_bf16 a[232:235], a[224:227], v[200:203], a[232:235]\n"
    "ds_read_b64_tr_b16 a[216:217], %[vb3] offset:16384\n"
    "ds_read_b64_tr_b16 a[218:219], %[vb3] offset:20480\n"
    "v_mfma_f32_16x16x32_bf16 a[236:239], a[224:227], v[208:211], a[236:239]\n"
    "s_waitcnt lgkmcnt(6)\n"
    "v_mfma_f32_16x16x32_bf16 v[76:79], a[192:195], v[0:3], 0\n"
    "buffer_load_dwordx4 %[vov], %[srv], s46 offen offset:128 sc0 sc1 lds\n"
    "v_mfma_f32_16x16x32_bf16 v[76:79], a[196:199], v[4:7], v[76:79]\n"
    "v_mfma_f32_16x16x32_bf16 v[140:143], a[192:195], v[8:11], 0\n"
    "v_mfma_f32_16x16x32_bf16 v[140:143], a[196:199], v[12:15], v[140:143]\n"
    "ds_read_b64_tr_b16 a[220:221], %[vb3] offset:24576\n"
    "ds_read_b64_tr_b16 a[222:223], %[vb3] offset:28672\n"
    "v_mfma_f32_16x16x32_bf16 v[204:207], a[192:195], v[16:19], 0\n"
    "ds_read_b64_tr_b16 a[192:193], %[vb0] offset:17408\n"
    "ds_read_b64_tr_b16 a[194:195], %[vb0] offset:21504\n"
    "v_mfma_f32_16x16x32_bf16 v[204:207], a[196:199], v[20:23], v[204:207]\n"
    "v_accvgpr_read_b32 v219, a228\n"
    "v_accvgpr_read_b32 v220, a232\n"
    "v_mfma_f32_16x16x32_bf16 v[84:87], a[200:203], v[0:3], 0\n"
    "v_accvgpr_read_b32 v221, a236\n"
    "v_mfma_f32_16x16x32_bf16 v[84:87], a[204:207], v[4:7], v[84:87]\n"
    "v_rcp_f32 v219, v219\n"
    "v_mfma_f32_16x16x32_bf16 v[148:151], a[200:203], v[8:11], 0\n"
    "v_rcp_f32 v220, v220\n"
    "v_rcp_f32 v221, v221\n"
    "v_mfma_f32_16x16x32_bf16 v[148:151], a[204:207], v[12:15], v[148:151]\n"
    "ds_read_b64_tr_b16 a[196:197], %[vb0] offset:25600\n"
    "ds_read_b64_tr_b16 a[198:199], %[vb0] offset:29696\n"
    "v_accvgpr_read_b32 v222, a64\n"
    "v_mfma_f32_16x16x32_bf16 v[212:215], a[200:203], v[16:19], 0\n"
    "ds_read_b64_tr_b16 a[200:201], %[vb1] offset:17408\n"
    "ds_read_b64_tr_b16 a[202:203], %[vb1] offset:21504\n"
    "v_accvgpr_read_b32 v223, a65\n"
    "v_mfma_f32_16x16x32_bf16 v[212:215], a[204:207], v[20:23], v[212:215]\n"
    "s_waitcnt lgkmcnt(8)\n"
    "v_accvgpr_read_b32 v224, a66\n"
    "v_accvgpr_read_b32 v225, a67\n"
    "v_mfma_f32_16x16x32_bf16 v[92:95], a[208:211], v[0:3], 0\n"
    "v_mul_f32 v222, v221, v222\n"
    "v_mul_f32 v223, v221, v223\n"
    "v_mfma_f32_16x16x32_bf16 v[92:95], a[212:215], v[4:7], v[92:95]\n"
    "v_mul_f32 v224, v221, v224\n"
    "v_mul_f32 v225, v221, v225\n"
    "v_mfma_f32_16x16x32_bf16 v[156:159], a[208:211], v[8:11], 0\n"
    "v_cvt_pk_bf16_f32 v222, v222, v223\n"
    "v_cvt_pk_bf16_f32 v223, v224, v225\n"
    "v_mfma_f32_16x16x32_bf16 v[156:159], a[212:215], v[12:15], v[156:159]\n"
    "ds_read_b64_tr_b16 a[204:205], %[vb1] offset:25600\n"
    "ds_read_b64_tr_b16 a[206:207], %[vb1] offset:29696\n"
    "v_mfma_f32_16x16x32_bf16 a[168:171], a[208:211], v[16:19], 0\n"
    "ds_read_b64_tr_b16 a[208:209], %[vb2] offset:17408\n"
    "ds_read_b64_tr_b16 a[210:211], %[vb2] offset:21504\n"
    "v_mfma_f32_16x16x32_bf16 a[168:171], a[212:215], v[20:23], a[168:171]\n"
    "ds_write_b64 %[stw], v[222:223] offset:0\n"
    "v_mfma_f32_16x16x32_bf16 v[100:103], a[216:219], v[0:3], 0\n"
    "s_waitcnt lgkmcnt(8)\n"
    "v_accvgpr_read_b32 v226, a68\n"
    "v_accvgpr_read_b32 v227, a69\n"
    "v_mfma_f32_16x16x32_bf16 v[100:103], a[220:223], v[4:7], v[100:103]\n"
    "v_accvgpr_read_b32 v228, a70\n"
    "v_accvgpr_read_b32 v229, a71\n"
    "v_mfma_f32_16x16x32_bf16 v[164:167], a[216:219], v[8:11], 0\n"
    "v_mul_f32 v226, v221, v226\n"
    "v_mul_f32 v227, v221, v227\n"
    "v_mfma_f32_16x16x32_bf16 v[164:167], a[220:223], v[12:15], v[164:167]\n"
    "ds_read_b64_tr_b16 a[212:213], %[vb2] offset:25600\n"
    "ds_read_b64_tr_b16 a[214:215], %[vb2] offset:29696\n"
    "v_mfma_f32_16x16x32_bf16 a[172:175], a[216:219], v[16:19], 0\n"
    "ds_read_b64_tr_b16 a[216:217], %[vb3] offset:17408\n"
    "ds_read_b64_tr_b16 a[218:219], %[vb3] offset:21504\n"
    "v_mfma_f32_16x16x32_bf16 a[172:175], a[220:223], v[20:23], a[172:175]\n"
    "v_mul_f32 v228, v221, v228\n"
    "v_mul_f32 v229, v221, v229\n"
    "v_mfma_f32_16x16x32_bf16 v[108:111], a[192:195], v[0:3], 0\n"
    "s_waitcnt lgkmcnt(9)\n"
    "v_cvt_pk_bf16_f32 v226, v226, v227\n"
    "v_cvt_pk_bf16_f32 v227, v228, v229\n"
    "v_mfma_f32_16x16x32_bf16 v[108:111], a[196:199], v[4:7], v[108:111]\n"
    "ds_write_b64 %[stw], v[226:227] offset:32\n"
    "v_mfma_f32_16x16x32_bf16 v[172:175], a[192:195], v[8:11], 0\n"
    "v_accvgpr_read_b32 v222, a72\n"
    "v_accvgpr_read_b32 v223, a73\n"
    "v_mfma_f32_16x16x32_bf16 v[172:175], a[196:199], v[12:15], v[172:175]\n"
    "s_cbranch_vccnz .Lpad28_%=\n"
    ".Lret28_%=:\n"
    "ds_read_b64_tr_b16 a[220:221], %[vb3] offset:25600\n"
    "ds_read_b64_tr_b16 a[222:223], %[vb3] offset:29696\n"
    "v_mfma_f32_16x16x32_bf16 a[176:179], a[192:195], v[16:19], 0\n"
    "s_waitcnt vmcnt(20)\n"
    "s_barrier\n"
    "v_add_u32 %[vb0], 0x10000, %[vb0]\n"
    "v_add_u32 %[vb1], 0x10000, %[vb1]\n"
    "v_mfma_f32_16x16x32_bf16 a[176:179], a[196:199], v[20:23], a[176:179]\n"
    "v_add_u32 %[vb2], 0x10000, %[vb2]\n"
    "v_add_u32 %[vb3], 0x10000, %[vb3]\n"
    "v_mfma_f32_16x16x32_bf16 v[116:119], a[200:203], v[0:3], 0\n"
    "s_waitcnt lgkmcnt(6)\n"
    "ds_read_b64_tr_b16 a[192:193], %[vb0] offset:32768\n"
    "ds_read_b64_tr_b16 a[194:195], %[vb0] offset:36864\n"
    "v_mfma_f32_16x16x32_bf16 v[116:119], a[204:207], v[4:7], v[116:119]\n"
    "s_add_u32 s46, s38, 0x28100\n"
    "v_accvgpr_read_b32 v224, a74\n"
    "v_accvgpr_read_b32 v225, a75\n"
    "v_mfma_f32_16x16x32_bf16 v[180:183], a[200:203], v[8:11], 0\n"
    "s_add_u32 m0, s42, 0x1c000\n"
    "v_mul_f32 v222, v221, v222\n"
    "v_mul_f32 v223, v221, v223\n"
    "v_mfma_f32_16x16x32_bf16 v[180:183], a[204:207], v[12:15], v[180:183]\n"
    "buffer_load_dwordx4 %[vov], %[srv], s46 offen sc0 sc1 lds\n"
    "v_mfma_f32_16x16x32_bf16 a[180:183], a[200:203], v[16:19], 0\n"
    "s_add_u32 m0, s42, 0x1c380\n"
    "ds_read_b64_tr_b16 a[196:197], %[vb0] offset:40960\n"
    "ds_read_b64_tr_b16 a[198:199], %[vb0] offset:45056\n"
    "v_mfma_f32_16x16x32_bf16 a[180:183], a[204:207], v[20:23], a[180:183]\n"
    "ds_read_b64_tr_b16 a[200:201], %[vb1] offset:32768\n"
    "ds_read_b64_tr_b16 a[202:203], %[vb1] offset:36864\n"
    "v_mfma_f32_16x16x32_bf16 v[124:127], a[208:211], v[0:3], 0\n"
    "s_waitcnt lgkmcnt(7)\n"
    "v_mul_f32 v224, v221, v224\n"
    "v_mul_f32 v225, v221, v225\n"
    "v_mfma_f32_16x16x32_bf16 v[124:127], a[212:215], v[4:7], v[124:127]\n"
    "buffer_load_dwordx4 %[vov], %[srv], s46 offen offset:128 sc0 sc1 lds\n"
    "v_mfma_f32_16x16x32_bf16 v[188:191], a[208:211], v[8:11], 0\n"
    "v_cvt_pk_bf16_f32 v222, v222, v223\n"
    "v_cvt_pk_bf16_f32 v223, v224, v225\n"
    "v_mfma_f32_16x16x32_bf16 v[188:191], a[212:215], v[12:15], v[188:191]\n"
    "ds_write_b64 %[stw], v[222:223] offset:64\n"
    "v_mfma_f32_16x16x32_bf16 a[184:187], a[208:211], v[16:19], 0\n"
    "s_add_u32 s46, s38, 0x29100\n"
    "ds_read_b64_tr_b16 a[204:205], %[vb1] offset:40960\n"
    "ds_read_b64_tr_b16 a[206:207], %[vb1] offset:45056\n"
    "v_mfma_f32_16x16x32_bf16 a[184:187], a[212:215], v[20:23], a[184:187]\n"
    "s_add_u32 m0, s42, 0x1c800\n"
    "ds_read_b64_tr_b16 a[208:209], %[vb2] offset:32768\n"
    "ds_read_b64_tr_b16 a[210:211], %[vb2] offset:36864\n"
    "v_accvgpr_read_b32 v226, a76\n"
    "v_mfma_f32_16x16x32_bf16 v[132:135], a[216:219], v[0:3], 0\n"
    "s_waitcnt lgkmcnt(9)\n"
    "v_accvgpr_read_b32 v227, a77\n"
    "v_accvgpr_read_b32 v228, a78\n"
    "v_mfma_f32_16x16x32_bf16 v[132:135], a[220:223], v[4:7], v[132:135]\n"
    "buffer_load_dwordx4 %[vov], %[srv], s46 offen sc0 sc1 lds\n"
    "v_mfma_f32_16x16x32_bf16 v[196:199], a[216:219], v[8:11], 0\n"
    "v_accvgpr_read_b32 v229, a79\n"
    "v_mul_f32 v226, v221, v226\n"
    "v_mfma_f32_16x16x32_bf16 v[196:199], a[220:223], v[12:15], v[196:199]\n"
    "ds_read_b64_tr_b16 a[212:213], %[vb2] offset:40960\n"
    "ds_read_b64_tr_b16 a[214:215], %[vb2] offset:45056\n"
    "v_mul_f32 v227, v221, v227\n"
    "v_mfma_f32_16x16x32_bf16 a[188:191], a[216:219], v[16:19], 0\n"
    "s_add_u32 m0, s42, 0x1cb80\n"
    "ds_read_b64_tr_b16 a[216:217], %[vb3] offset:32768\n"
    "ds_read_b64_tr_b16 a[218:219], %[vb3] offset:36864\n"
    "v_mul_f32 v228, v221, v228\n"
    "v_mfma_f32_16x16x32_bf16 a[188:191], a[220:223], v[20:23], a[188:191]\n"
    "v_mul_f32 v229, v221, v229\n"
    "v_cvt_pk_bf16_f32 v226, v226, v227\n"
    "v_mfma_f32_16x16x32_bf16 v[76:79], a[192:195], v[24:27], v[76:79]\n"
    "s_waitcnt lgkmcnt(8)\n"
    "v_cvt_pk_bf16_f32 v227, v228, v229\n"
    "v_mfma_f32_16x16x32_bf16 v[76:79], a[196:199], v[28:31], v[76:79]\n"
    "ds_write_b64 %[stw], v[226:227] offset:96\n"
    "v_mfma_f32_16x16x32_bf16 v[140:143], a[192:195], v[32:35], v[140:143]\n"
    "buffer_load_dwordx4 %[vov], %[srv], s46 offen offset:128 sc0 sc1 lds\n"
    "v_mfma_f32_16x16x32_bf16 v[140:143], a[196:199], v[36:39], v[140:143]\n"
    "ds_read_b64_tr_b16 a[220:221], %[vb3] offset:40960\n"
    "ds_read_b64_tr_b16 a[222:223], %[vb3] offset:45056\n"
    "v_mfma_f32_16x16x32_bf16 v[204:207], a[192:195], v[40:43], v[204:207]\n"
    "ds_read_b64_tr_b16 a[192:193], %[vb0] offset:33792\n"
    "ds_read_b64_tr_b16 a[194:195], %[vb0] offset:37888\n"
    "v_accvgpr_read_b32 v222, a80\n"
    "v_accvgpr_read_b32 v223, a81\n"
    "v_mfma_f32_16x16x32_bf16 v[204:207], a[196:199], v[44:47], v[204:207]\n"
    "v_accvgpr_read_b32 v224, a82\n"
    "v_accvgpr_read_b32 v225, a83\n"
    "v_mfma_f32_16x16x32_bf16 v[84:87], a[200:203], v[24:27], v[84:87]\n"
    "s_waitcnt lgkmcnt(7)\n"
    "v_mul_f32 v222, v221, v222\n"
    "v_mul_f32 v223, v221, v223\n"
    "v_mfma_f32_16x16x32_bf16 v[84:87], a[204:207], v[28:31], v[84:87]\n"
    "v_mul_f32 v224, v221, v224\n"
    "v_mul_f32 v225, v221, v225\n"
    "v_mfma_f32_16x16x32_bf16 v[148:151], a[200:203], v[32:35], v[148:151]\n"
    "v_cvt_pk_bf16_f32 v222, v222, v223\n"
    "v_cvt_pk_bf16_f32 v223, v224, v225\n"
    "v_mfma_f32_16x16x32_bf16 v[148:151], a[204:207], v[36:39], v[148:151]\n"
    "ds_read_b64_tr_b16 a[196:197], %[vb0] offset:41984\n"
    "ds_read_b64_tr_b16 a[198:199], %[vb0] offset:46080\n"
    "v_mfma_f32_16x16x32_bf16 v[212:215], a[200:203], v[40:43], v[212:215]\n"
    "ds_read_b64_tr_b16 a[200:201], %[vb1] offset:33792\n"
    "ds_read_b64_tr_b16 a[202:203], %[vb1] offset:37888\n"
    "v_mfma_f32_16x16x32_bf16 v[212:215], a[204:207], v[44:47], v[212:215]\n"
    "s_cbranch_vccnz .Lpad29_%=\n"
    ".Lret29_%=:\n"
    "ds_write_b64 %[stw], v[222:223] offset:128\n"
    "v_mfma_f32_16x16x32_bf16 v[92:95], a[208:211], v[24:27], v[92:95]\n"
    "v_accvgpr_read_b32 v226, a84\n"
    "v_accvgpr_read_b32 v227, a85\n"
    "v_mfma_f32_16x16x32_bf16 v[92:95], a[212:215], v[28:31], v[92:95]\n"
    "v_accvgpr_read_b32 v228, a86\n"
    "v_accvgpr_read_b32 v229, a87\n"
    "v_mfma_f32_16x16x32_bf16 v[156:159], a[208:211], v[32:35], v[156:159]\n"
    "v_mul_f32 v226, v221, v226\n"
    "v_mul_f32 v227, v221, v227\n"
    "v_mfma_f32_16x16x32_bf16 v[156:159], a[212:215], v[36:39], v[156:159]\n"
    "ds_read_b64_tr_b16 a[204:205], %[vb1] offset:41984\n"
    "ds_read_b64_tr_b16 a[206:207], %[vb1] offset:46080\n"
    "v_mfma_f32_16x16x32_bf16 a[168:171], a[208:211], v[40:43], a[168:171]\n"
    "ds_read_b64_tr_b16 a[208:209], %[vb2] offset:33792\n"
    "s_waitcnt lgkmcnt(8)\n"
    "ds_read_b64_tr_b16 a[210:211], %[vb2] offset:37888\n"
    "v_mfma_f32_16x16x32_bf16 a[168:171], a[212:215], v[44:47], a[168:171]\n"
    "v_mul_f32 v228, v221, v228\n"
    "v_mul_f32 v229, v221, v229\n"
    "v_mfma_f32_16x16x32_bf16 v[100:103], a[216:219], v[24:27], v[100:103]\n"
    "v_cvt_pk_bf16_f32 v226, v226, v227\n"
    "v_cvt_pk_bf16_f32 v227, v228, v229\n"
    "v_mfma_f32_16x16x32_bf16 v[100:103], a[220:223], v[28:31], v[100:103]\n"
    "ds_write_b64 %[stw], v[226:227] offset:160\n"
    "v_mfma_f32_16x16x32_bf16 v[164:167], a[216:219], v[32:35], v[164:167]\n"
    "v_accvgpr_read_b32 v222, a88\n"
    "v_accvgpr_read_b32 v223, a89\n"
    "v_mfma_f32_16x16x32_bf16 v[164:167], a[220:223], v[36:39], v[164:167]\n"
    "ds_read_b64_tr_b16 a[212:213], %[vb2] offset:41984\n"
    "ds_read_b64_tr_b16 a[214:215], %[vb2] offset:46080\n"
    "v_mfma_f32_16x16x32_bf16 a[172:175], a[216:219], v[40:43], a[172:175]\n"
    "ds_read_b64_tr_b16 a[216:217], %[vb3] offset:33792\n"
    "ds_read_b64_tr_b16 a[218:219], %[vb3] offset:37888\n"
    "v_mfma_f32_16x16x32_bf16 a[172:175], a[220:223], v[44:47], a[172:175]\n"
    "v_accvgpr_read_b32 v224, a90\n"
    "v_accvgpr_read_b32 v225, a91\n"
    "v_mfma_f32_16x16x32_bf16 v[108:111], a[192:195], v[24:27], v[108:111]\n"
    "s_waitcnt lgkmcnt(9)\n"
    "v_mul_f32 v222, v221, v222\n"
    "v_mul_f32 v223, v221, v223\n"
    "v_mfma_f32_16x16x32_bf16 v[108:111], a[196:199], v[28:31], v[108:111]\n"
    "v_mul_f32 v224, v221, v224\n"
    "v_mul_f32 v225, v221, v225\n"
    "v_mfma_f32_16x16x32_bf16 v[172:175], a[192:195], v[32:35], v[172:175]\n"
    "v_cvt_pk_bf16_f32 v222, v222, v223\n"
    "v_cvt_pk_bf16_f32 v223, v224, v225\n"
    "v_mfma_f32_16x16x32_bf16 v[172:175], a[196:199], v[36:39], v[172:175]\n"
    "ds_read_b64_tr_b16 a[220:221], %[vb3] offset:41984\n"
    "ds_read_b64_tr_b16 a[222:223], %[vb3] offset:46080\n"
    "v_mfma_f32_16x16x32_bf16 a[176:179], a[192:195], v[40:43], a[176:179]\n"
    "s_waitcnt vmcnt(16)\n"
    "s_barrier\n"
    "ds_read_b64_tr_b16 a[192:193], %[vb0] offset:0\n"
    "ds_read_b64_tr_b16 a[194:195], %[vb0] offset:4096\n"
    "v_mfma_f32_16x16x32_bf16 a[176:179], a[196:199], v[44:47], a[176:179]\n"
    "ds_write_b64 %[stw], v[222:223] offset:192\n"
    "v_mfma_f32_16x16x32_bf16 v[116:119], a[200:203], v[24:27], v[116:119]\n"
    "s_waitcnt lgkmcnt(8)\n"
    "v_accvgpr_read_b32 v226, a92\n"
    "v_accvgpr_read_b32 v227, a93\n"
    "v_mfma_f32_16x16x32_bf16 v[116:119], a[204:207], v[28:31], v[116:119]\n"
    "s_add_u32 s46, s37, 0x0\n"
    "v_accvgpr_read_b32 v228, a94\n"
    "v_accvgpr_read_b32 v229, a95\n"
    "v_mfma_f32_16x16x32_bf16 v[180:183], a[200:203], v[32:35], v[180:183]\n"
    "s_add_u32 m0, s42, 0x4000\n"
    "v_mul_f32 v226, v221, v226\n"
    "v_mul_f32 v227, v221, v227\n"
    "v_mfma_f32_16x16x32_bf16 v[180:183], a[204:207], v[36:39], v[180:183]\n"
    "buffer_load_dwordx4 %[vok], %[srk], s46 offen nt lds\n"
    "v_mfma_f32_16x16x32_bf16 a[180:183], a[200:203], v[40:43], a[180:183]\n"
    "s_add_u32 m0, s42, 0x4380\n"
    "ds_read_b64_tr_b16 a[196:197], %[vb0] offset:8192\n"
    "ds_read_b64_tr_b16 a[198:199], %[vb0] offset:12288\n"
    "v_mfma_f32_16x16x32_bf16 a[180:183], a[204:207], v[44:47], a[180:183]\n"
    "ds_read_b64_tr_b16 a[200:201], %[vb1] offset:0\n"
    "ds_read_b64_tr_b16 a[202:203], %[vb1] offset:4096\n"
    "v_mfma_f32_16x16x32_bf16 v[124:127], a[208:211], v[24:27], v[124:127]\n"
    "s_waitcnt lgkmcnt(8)\n"
    "v_mul_f32 v228, v221, v228\n"
    "v_mul_f32 v229, v221, v229\n"
    "v_mfma_f32_16x16x32_bf16 v[124:127], a[212:215], v[28:31], v[124:127]\n"
    "buffer_load_dwordx4 %[vok], %[srk], s46 offen offset:128 nt lds\n"
    "v_mfma_f32_16x16x32_bf16 v[188:191], a[208:211], v[32:35], v[188:191]\n"
    "v_cvt_pk_bf16_f32 v226, v226, v227\n"
    "v_cvt_pk_bf16_f32 v227, v228, v229\n"
    "v_mfma_f32_16x16x32_bf16 v[188:191], a[212:215], v[36:39], v[188:191]\n"
    "ds_read_b64_tr_b16 a[204:205], %[vb1] offset:8192\n"
    "ds_read_b64_tr_b16 a[206:207], %[vb1] offset:12288\n"
    "v_mfma_f32_16x16x32_bf16 a[184:187], a[208:211], v[40:43], a[184:187]\n"
    "s_add_u32 m0, s42, 0x4700\n"
    "ds_read_b64_tr_b16 a[208:209], %[vb2] offset:0\n"
    "ds_read_b64_tr_b16 a[210:211], %[vb2] offset:4096\n"
    "v_mfma_f32_16x16x32_bf16 a[184:187], a[212:215], v[44:47], a[184:187]\n"
    "ds_write_b64 %[stw], v[226:227] offset:224\n"
    "v_mfma_f32_16x16x32_bf16 v[132:135], a[216:219], v[24:27], v[132:135]\n"
    "s_waitcnt lgkmcnt(9)\n"
    "ds_read_b128 a[240:243], %[str] offset:0\n"
    "v_mfma_f32_16x16x32_bf16 v[132:135], a[220:223], v[28:31], v[132:135]\n"
    "s_cbranch_vccnz .Lpad30_%=\n"
    ".Lret30_%=:\n"
    "buffer_load_dwordx4 %[vok], %[srk], s46 offen offset:256 nt lds\n"
    "v_mfma_f32_16x16x32_bf16 v[196:199], a[216:219], v[32:35], v[196:199]\n"
    "ds_read_b128 a[244:247], %[str] offset:1088\n"
    "v_mfma_f32_16x16x32_bf16 v[196:199], a[220:223], v[36:39], v[196:199]\n"
    "ds_read_b64_tr_b16 a[212:213], %[vb2] offset:8192\n"
    "ds_read_b64_tr_b16 a[214:215], %[vb2] offset:12288\n"
    "v_mfma_f32_16x16x32_bf16 a[188:191], a[216:219], v[40:43], a[188:191]\n"
    "s_add_u32 m0, s42, 0x4a80\n"
    "ds_read_b128 a[248:251], %[str] offset:2176\n"
    "ds_read_b64_tr_b16 a[216:217], %[vb3] offset:0\n"
    "v_mfma_f32_16x16x32_bf16 a[188:191], a[220:223], v[44:47], a[188:191]\n"
    "s_waitcnt lgkmcnt(8)\n"
    "ds_read_b64_tr_b16 a[218:219], %[vb3] offset:4096\n"
    "v_mfma_f32_16x16x32_bf16 v[76:79], a[192:195], v[48:51], v[76:79]\n"
    "ds_read_b128 a[252:255], %[str] offset:3264\n"
    "v_mfma_f32_16x16x32_bf16 v[76:79], a[196:199], v[52:55], v[76:79]\n"
    "s_waitcnt vmcnt(15)\n"
    "buffer_load_dwordx4 %[vok], %[srk], s46 offen offset:384 nt lds\n"
    "v_mfma_f32_16x16x32_bf16 v[140:143], a[192:195], v[56:59], v[140:143]\n"
    "v_add_u32 %[qb0], 0xffff0000, %[qb0]\n"
    "v_add_u32 %[qb1], 0xffff0000, %[qb1]\n"
    "v_mfma_f32_16x16x32_bf16 v[140:143], a[196:199], v[60:63], v[140:143]\n"
    "ds_read_b128 a[64:67], %[qb0] offset:32768\n"
    "ds_read_b128 a[68:71], %[qb1] offset:32768\n"
    "v_mfma_f32_16x16x32_bf16 v[204:207], a[192:195], v[64:67], v[204:207]\n"
    "s_add_u32 s47, s40, 0x4000\n"
    "ds_read_b64_tr_b16 a[220:221], %[vb3] offset:8192\n"
    "ds_read_b64_tr_b16 a[222:223], %[vb3] offset:12288\n"
    "v_mfma_f32_16x16x32_bf16 v[204:207], a[196:199], v[68:71], v[204:207]\n"
    "ds_read_b128 a[72:75], %[qb0] offset:33792\n"
    "s_waitcnt lgkmcnt(8)\n"
    "ds_read_b64_tr_b16 a[192:193], %[vb0] offset:1024\n"
    "v_mfma_f32_16x16x32_bf16 v[84:87], a[200:203], v[48:51], v[84:87]\n"
    "ds_read_b64_tr_b16 a[194:195], %[vb0] offset:5120\n"
    "ds_read_b128 a[76:79], %[qb1] offset:33792\n"
    "v_mfma_f32_16x16x32_bf16 v[84:87], a[204:207], v[52:55], v[84:87]\n"
    "ds_read_b128 a[80:83], %[qb0] offset:34816\n"
    "ds_read_b128 a[84:87], %[qb1] offset:34816\n"
    "v_mfma_f32_16x16x32_bf16 v[148:151], a[200:203], v[56:59], v[148:151]\n"
    "ds_read_b128 a[88:91], %[qb0] offset:35840\n"
    "ds_read_b128 a[92:95], %[qb1] offset:35840\n"
    "v_mfma_f32_16x16x32_bf16 v[148:151], a[204:207], v[60:63], v[148:151]\n"
    "s_waitcnt lgkmcnt(8)\n"
    "ds_read_b64_tr_b16 a[196:197], %[vb0] offset:9216\n"
    "v_mfma_f32_16x16x32_bf16 v[212:215], a[200:203], v[64:67], v[212:215]\n"
    "ds_read_b64_tr_b16 a[198:199], %[vb0] offset:13312\n"
    "ds_read_b64_tr_b16 a[200:201], %[vb1] offset:1024\n"
    "v_mfma_f32_16x16x32_bf16 v[212:215], a[204:207], v[68:71], v[212:215]\n"
    "ds_read_b64_tr_b16 a[202:203], %[vb1] offset:5120\n"
    "v_accvgpr_read_b32 v222, a0\n"
    "v_mfma_f32_16x16x32_bf16 v[92:95], a[208:211], v[48:51], v[92:95]\n"
    "v_accvgpr_read_b32 v223, a1\n"
    "v_accvgpr_read_b32 v224, a2\n"
    "v_mfma_f32_16x16x32_bf16 v[92:95], a[212:215], v[52:55], v[92:95]\n"
    "v_accvgpr_read_b32 v225, a3\n"
    "v_mul_f32 v222, v219, v222\n"
    "v_mfma_f32_16x16x32_bf16 v[156:159], a[208:211], v[56:59], v[156:159]\n"
    "buffer_store_dwordx4 a[240:243], %[vos], %[sro], s47 offen offset:0 sc0 sc1\n"
    "v_mfma_f32_16x16x32_bf16 v[156:159], a[212:215], v[60:63], v[156:159]\n"
    "ds_read_b64_tr_b16 a[204:205], %[vb1] offset:9216\n"
    "ds_read_b64_tr_b16 a[206:207], %[vb1] offset:13312\n"
    "v_mfma_f32_16x16x32_bf16 a[168:171], a[208:211], v[64:67], a[168:171]\n"
    "s_add_u32 s46, s37, 0x8000\n"
    "ds_read_b64_tr_b16 a[208:209], %[vb2] offset:1024\n"
    "s_waitcnt lgkmcnt(8)\n"
    "ds_read_b64_tr_b16 a[210:211], %[vb2] offset:5120\n"
    "v_mul_f32 v223, v219, v223\n"
    "v_mfma_f32_16x16x32_bf16 a[168:171], a[212:215], v[68:71], a[168:171]\n"
    "s_add_u32 m0, s42, 0xc000\n"
    "v_mul_f32 v224, v219, v224\n"
    "v_mul_f32 v225, v219, v225\n"
    "v_mfma_f32_16x16x32_bf16 v[100:103], a[216:219], v[48:51], v[100:103]\n"
    "s_waitcnt lgkmcnt(8)\n"
    "buffer_load_dwordx4 %[vok], %[srk], s46 offen nt lds\n"
    "v_cvt_pk_bf16_f32 v222, v222, v223\n"
    "v_mfma_f32_16x16x32_bf16 v[100:103], a[220:223], v[52:55], v[100:103]\n"
    "v_cvt_pk_bf16_f32 v223, v224, v225\n"
    "v_mfma_f32_16x16x32_bf16 v[164:167], a[216:219], v[56:59], v[164:167]\n"
    "s_add_u32 m0, s42, 0xc380\n"
    "ds_write_b64 %[stw], v[222:223] offset:0\n"
    "v_mfma_f32_16x16x32_bf16 v[164:167], a[220:223], v[60:63], v[164:167]\n"
    "ds_read_b64_tr_b16 a[212:213], %[vb2] offset:9216\n"
    "ds_read_b64_tr_b16 a[214:215], %[vb2] offset:13312\n"
    "v_mfma_f32_16x16x32_bf16 a[172:175], a[216:219], v[64:67], a[172:175]\n"
    "ds_read_b64_tr_b16 a[216:217], %[vb3] offset:1024\n"
    "ds_read_b64_tr_b16 a[218:219], %[vb3] offset:5120\n"
    "v_accvgpr_read_b32 v226, a4\n"
    "v_mfma_f32_16x16x32_bf16 a[172:175], a[220:223], v[68:71], a[172:175]\n"
    "buffer_load_dwordx4 %[vok], %[srk], s46 offen offset:128 nt lds\n"
    "v_accvgpr_read_b32 v227, a5\n"
    "v_mfma_f32_16x16x32_bf16 v[108:111], a[192:195], v[48:51], v[108:111]\n"
    "s_waitcnt lgkmcnt(6)\n"
    "v_accvgpr_read_b32 v228, a6\n"
    "v_accvgpr_read_b32 v229, a7\n"
    "v_mfma_f32_16x16x32_bf16 v[108:111], a[196:199], v[52:55], v[108:111]\n"
    "v_mul_f32 v226, v219, v226\n"
    "v_mul_f32 v227, v219, v227\n"
    "v_mfma_f32_16x16x32_bf16 v[172:175], a[192:195], v[56:59], v[172:175]\n"
    "v_mul_f32 v228, v219, v228\n"
    "v_mul_f32 v229, v219, v229\n"
    "v_mfma_f32_16x16x32_bf16 v[172:175], a[196:199], v[60:63], v[172:175]\n"
    "s_cbranch_vccnz .Lpad31_%=\n"
    ".Lret31_%=:\n"
    "ds_read_b64_tr_b16 a[220:221], %[vb3] offset:9216\n"
    "ds_read_b64_tr_b16 a[222:223], %[vb3] offset:13312\n"
    "v_mfma_f32_16x16x32_bf16 a[176:179], a[192:195], v[64:67], a[176:179]\n"
    "s_waitcnt vmcnt(15)\n"
    "s_barrier\n"
    "ds_read_b64_tr_b16 a[192:193], %[vb0] offset:16384\n"
    "ds_read_b64_tr_b16 a[194:195], %[vb0] offset:20480\n"
    "v_mfma_f32_16x16x32_bf16 a[176:179], a[196:199], v[68:71], a[176:179]\n"
    "v_cvt_pk_bf16_f32 v226, v226, v227\n"
    "v_cvt_pk_bf16_f32 v227, v228, v229\n"
    "v_mfma_f32_16x16x32_bf16 v[116:119], a[200:203], v[48:51], v[116:119]\n"
    "ds_write_b64 %[stw], v[226:227] offset:32\n"
    "v_mfma_f32_16x16x32_bf16 v[116:119], a[204:207], v[52:55], v[116:119]\n"
    "v_accvgpr_read_b32 v222, a8\n"
    "v_accvgpr_read_b32 v223, a9\n"
    "v_mfma_f32_16x16x32_bf16 v[180:183], a[200:203], v[56:59], v[180:183]\n"
    "v_accvgpr_read_b32 v224, a10\n"
    "v_accvgpr_read_b32 v225, a11\n"
    "v_mfma_f32_16x16x32_bf16 v[180:183], a[204:207], v[60:63], v[180:183]\n"
    "ds_read_b64_tr_b16 a[196:197], %[vb0] offset:24576\n"
    "ds_read_b64_tr_b16 a[198:199], %[vb0] offset:28672\n"
    "v_mfma_f32_16x16x32_bf16 a[180:183], a[200:203], v[64:67], a[180:183]\n"
    "s_add_u32 m0, s42, 0xc700\n"
    "ds_read_b64_tr_b16 a[200:201], %[vb1] offset:16384\n"
    "ds_read_b64_tr_b16 a[202:203], %[vb1] offset:20480\n"
    "v_mfma_f32_16x16x32_bf16 a[180:183], a[204:207], v[68:71], a[180:183]\n"
    "s_waitcnt lgkmcnt(9)\n"
    "buffer_store_dwordx4 a[244:247], %[vos], %[sro], s47 offen offset:2048 sc0 sc1\n"
    "v_mfma_f32_16x16x32_bf16 v[124:127], a[208:211], v[48:51], v[124:127]\n"
    "v_mul_f32 v222, v219, v222\n"
    "v_mul_f32 v223, v219, v223\n"
    "v_mfma_f32_16x16x32_bf16 v[124:127], a[212:215], v[52:55], v[124:127]\n"
    "buffer_load_dwordx4 %[vok], %[srk], s46 offen offset:256 nt lds\n"
    "v_mfma_f32_16x16x32_bf16 v[188:191], a[208:211], v[56:59], v[188:191]\n"
    "v_mul_f32 v224, v219, v224\n"
    "v_mul_f32 v225, v219, v225\n"
    "v_mfma_f32_16x16x32_bf16 v[188:191], a[212:215], v[60:63], v[188:191]\n"
    "ds_read_b64_tr_b16 a[204:205], %[vb1] offset:24576\n"
    "ds_read_b64_tr_b16 a[206:207], %[vb1] offset:28672\n"
    "v_mfma_f32_16x16x32_bf16 a[184:187], a[208:211], v[64:67], a[184:187]\n"
    "s_add_u32 m0, s42, 0xca80\n"
    "ds_read_b64_tr_b16 a[208:209], %[vb2] offset:16384\n"
    "ds_read_b64_tr_b16 a[210:211], %[vb2] offset:20480\n"
    "v_cvt_pk_bf16_f32 v222, v222, v223\n"
    "v_cvt_pk_bf16_f32 v223, v224, v225\n"
    "v_mfma_f32_16x16x32_bf16 a[184:187], a[212:215], v[68:71], a[184:187]\n"
    "ds_write_b64 %[stw], v[222:223] offset:64\n"
    "v_mfma_f32_16x16x32_bf16 v[132:135], a[216:219], v[48:51], v[132:135]\n"
    "s_waitcnt lgkmcnt(8)\n"
    "v_accvgpr_read_b32 v226, a12\n"
    "v_accvgpr_read_b32 v227, a13\n"
    "v_mfma_f32_16x16x32_bf16 v[132:135], a[220:223], v[52:55], v[132:135]\n"
    "buffer_load_dwordx4 %[vok], %[srk], s46 offen offset:384 nt lds\n"
    "v_mfma_f32_16x16x32_bf16 v[196:199], a[216:219], v[56:59], v[196:199]\n"
    "v_accvgpr_read_b32 v228, a14\n"
    "v_accvgpr_read_b32 v229, a15\n"
    "v_mfma_f32_16x16x32_bf16 v[196:199], a[220:223], v[60:63], v[196:199]\n"
    "ds_read_b64_tr_b16 a[212:213], %[vb2] offset:24576\n"
    "ds_read_b64_tr_b16 a[214:215], %[vb2] offset:28672\n"
    "v_mfma_f32_16x16x32_bf16 a[188:191], a[216:219], v[64:67], a[188:191]\n"
    "ds_read_b64_tr_b16 a[216:217], %[vb3] offset:16384\n"
    "ds_read_b64_tr_b16 a[218:219], %[vb3] offset:20480\n"
    "v_mfma_f32_16x16x32_bf16 a[188:191], a[220:223], v[68:71], a[188:191]\n"
    "v_mul_f32 v226, v219, v226\n"
    "v_mul_f32 v227, v219, v227\n"
    "v_mfma_f32_16x16x32_bf16 v[76:79], a[192:195], v[72:75], v[76:79]\n"
    "s_waitcnt lgkmcnt(8)\n"
    "v_mul_f32 v228, v219, v228\n"
    "v_mul_f32 v229, v219, v229\n"
    "v_mfma_f32_16x16x32_bf16 v[76:79], a[196:199], v[96:99], v[76:79]\n"
    "v_cvt_pk_bf16_f32 v226, v226, v227\n"
    "v_cvt_pk_bf16_f32 v227, v228, v229\n"
    "v_mfma_f32_16x16x32_bf16 v[140:143], a[192:195], v[80:83], v[140:143]\n"
    "ds_write_b64 %[stw], v[226:227] offset:96\n"
    "v_mfma_f32_16x16x32_bf16 v[140:143], a[196:199], v[104:107], v[140:143]\n"
    "ds_read_b64_tr_b16 a[220:221], %[vb3] offset:24576\n"
    "ds_read_b64_tr_b16 a[222:223], %[vb3] offset:28672\n"
    "v_mfma_f32_16x16x32_bf16 v[204:207], a[192:195], v[88:91], v[204:207]\n"
    "s_add_u32 s47, s40, 0x5000\n"
    "ds_read_b64_tr_b16 a[192:193], %[vb0] offset:17408\n"
    "ds_read_b64_tr_b16 a[194:195], %[vb0] offset:21504\n"
    "v_mfma_f32_16x16x32_bf16 v[204:207], a[196:199], v[112:115], v[204:207]\n"
    "s_add_u32 s46, s37, 0x4000\n"
    "v_accvgpr_read_b32 v222, a16\n"
    "v_accvgpr_read_b32 v223, a17\n"
    "v_mfma_f32_16x16x32_bf16 v[84:87], a[200:203], v[72:75], v[84:87]\n"
    "s_waitcnt lgkmcnt(9)\n"
    "v_accvgpr_read_b32 v224, a18\n"
    "v_accvgpr_read_b32 v225, a19\n"
    "v_mfma_f32_16x16x32_bf16 v[84:87], a[204:207], v[96:99], v[84:87]\n"
    "s_add_u32 m0, s42, 0x8000\n"
    "v_mul_f32 v222, v219, v222\n"
    "v_mul_f32 v223, v219, v223\n"
    "v_mfma_f32_16x16x32_bf16 v[148:151], a[200:203], v[80:83], v[148:151]\n"
    "buffer_load_dwordx4 %[vok], %[srk], s46 offen nt lds\n"
    "v_mfma_f32_16x16x32_bf16 v[148:151], a[204:207], v[104:107], v[148:151]\n"
    "ds_read_b64_tr_b16 a[196:197], %[vb0] offset:25600\n"
    "ds_read_b64_tr_b16 a[198:199], %[vb0] offset:29696\n"
    "v_mfma_f32_16x16x32_bf16 v[212:215], a[200:203], v[88:91], v[212:215]\n"
    "ds_read_b64_tr_b16 a[200:201], %[vb1] offset:17408\n"
    "ds_read_b64_tr_b16 a[202:203], %[vb1] offset:21504\n"
    "v_mfma_f32_16x16x32_bf16 v[212:215], a[204:207], v[112:115], v[212:215]\n"
    "s_cbranch_vccnz .Lpad32_%=\n"
    ".Lret32_%=:\n"
    "s_add_u32 m0, s42, 0x8380\n"
    "v_mul_f32 v224, v219, v224\n"
    "v_mul_f32 v225, v219, v225\n"
    "v_mfma_f32_16x16x32_bf16 v[92:95], a[208:211], v[72:75], v[92:95]\n"
    "s_waitcnt lgkmcnt(8)\n"
    "v_cvt_pk_bf16_f32 v222, v222, v223\n"
    "v_cvt_pk_bf16_f32 v223, v224, v225\n"
    "v_mfma_f32_16x16x32_bf16 v[92:95], a[212:215], v[96:99], v[92:95]\n"
    "ds_write_b64 %[stw], v[222:223] offset:128\n"
    "v_mfma_f32_16x16x32_bf16 v[156:159], a[208:211], v[80:83], v[156:159]\n"
    "buffer_load_dwordx4 %[vok], %[srk], s46 offen offset:128 nt lds\n"
    "v_mfma_f32_16x16x32_bf16 v[156:159], a[212:215], v[104:107], v[156:159]\n"
    "ds_read_b64_tr_b16 a[204:205], %[vb1] offset:25600\n"
    "ds_read_b64_tr_b16 a[206:207], %[vb1] offset:29696\n"
    "v_mfma_f32_16x16x32_bf16 a[168:171], a[208:211], v[88:91], a[168:171]\n"
    "ds_read_b64_tr_b16 a[208:209], %[vb2] offset:17408\n"
    "ds_read_b64_tr_b16 a[210:211], %[vb2] offset:21504\n"
    "v_accvgpr_read_b32 v226, a20\n"
    "v_mfma_f32_16x16x32_bf16 a[168:171], a[212:215], v[112:115], a[168:171]\n"
    "s_add_u32 m0, s42, 0x8700\n"
    "v_accvgpr_read_b32 v227, a21\n"
    "v_accvgpr_read_b32 v228, a22\n"
    "v_mfma_f32_16x16x32_bf16 v[100:103], a[216:219], v[72:75], v[100:103]\n"
    "s_waitcnt lgkmcnt(9)\n"
    "buffer_store_dwordx4 a[248:251], %[vos], %[sro], s47 offen offset:0 sc0 sc1\n"
    "v_mfma_f32_16x16x32_bf16 v[100:103], a[220:223], v[96:99], v[100:103]\n"
    "v_accvgpr_read_b32 v229, a23\n"
    "v_mul_f32 v226, v219, v226\n"
    "v_mfma_f32_16x16x32_bf16 v[164:167], a[216:219], v[80:83], v[164:167]\n"
    "buffer_load_dwordx4 %[vok], %[srk], s46 offen offset:256 nt lds\n"
    "v_mfma_f32_16x16x32_bf16 v[164:167], a[220:223], v[104:107], v[164:167]\n"
    "ds_read_b64_tr_b16 a[212:213], %[vb2] offset:25600\n"
    "ds_read_b64_tr_b16 a[214:215], %[vb2] offset:29696\n"
    "v_mul_f32 v227, v219, v227\n"
    "v_mul_f32 v228, v219, v228\n"
    "v_mfma_f32_16x16x32_bf16 a[172:175], a[216:219], v[88:91], a[172:175]\n"
    "ds_read_b64_tr_b16 a[216:217], %[vb3] offset:17408\n"
    "ds_read_b64_tr_b16 a[218:219], %[vb3] offset:21504\n"
    "v_mul_f32 v229, v219, v229\n"
    "v_mfma_f32_16x16x32_bf16 a[172:175], a[220:223], v[112:115], a[172:175]\n"
    "s_add_u32 m0, s42, 0x8a80\n"
    "v_cvt_pk_bf16_f32 v226, v226, v227\n"
    "v_cvt_pk_bf16_f32 v227, v228, v229\n"
    "v_mfma_f32_16x16x32_bf16 v[108:111], a[192:195], v[72:75], v[108:111]\n"
    "s_waitcnt lgkmcnt(6)\n"
    "ds_write_b64 %[stw], v[226:227] offset:160\n"
    "v_mfma_f32_16x16x32_bf16 v[108:111], a[196:199], v[96:99], v[108:111]\n"
    "v_accvgpr_read_b32 v222, a24\n"
    "v_accvgpr_read_b32 v223, a25\n"
    "v_mfma_f32_16x16x32_bf16 v[172:175], a[192:195], v[80:83], v[172:175]\n"
    "buffer_load_dwordx4 %[vok], %[srk], s46 offen offset:384 nt lds\n"
    "v_mfma_f32_16x16x32_bf16 v[172:175], a[196:199], v[104:107], v[172:175]\n"
    "ds_read_b64_tr_b16 a[220:221], %[vb3] offset:25600\n"
    "ds_read_b64_tr_b16 a[222:223], %[vb3] offset:29696\n"
    "v_mfma_f32_16x16x32_bf16 a[176:179], a[192:195], v[88:91], a[176:179]\n"
    "s_waitcnt vmcnt(19)\n"
    "s_barrier\n"
    "v_accvgpr_read_b32 v224, a26\n"
    "v_accvgpr_read_b32 v225, a27\n"
    "v_mfma_f32_16x16x32_bf16 a[176:179], a[196:199], v[112:115], a[176:179]\n"
    "v_mul_f32 v222, v219, v222\n"
    "v_add_u32 %[vb0], 0xffff0000, %[vb0]\n"
    "v_mfma_f32_16x16x32_bf16 v[116:119], a[200:203], v[72:75], v[116:119]\n"
    "v_add_u32 %[vb1], 0xffff0000, %[vb1]\n"
    "v_add_u32 %[vb2], 0xffff0000, %[vb2]\n"
    "v_add_u32 %[vb3], 0xffff0000, %[vb3]\n"
    "ds_read_b64_tr_b16 a[192:193], %[vb0] offset:0\n"
    "ds_read_b64_tr_b16 a[194:195], %[vb0] offset:4096\n"
    "v_mfma_f32_16x16x32_bf16 v[116:119], a[204:207], v[96:99], v[116:119]\n"
    "v_mul_f32 v223, v219, v223\n"
    "v_mul_f32 v224, v219, v224\n"
    "v_mfma_f32_16x16x32_bf16 v[180:183], a[200:203], v[80:83], v[180:183]\n"
    "v_mul_f32 v225, v219, v225\n"
    "v_cvt_pk_bf16_f32 v222, v222, v223\n"
    "v_mfma_f32_16x16x32_bf16 v[180:183], a[204:207], v[104:107], v[180:183]\n"
    "ds_read_b64_tr_b16 a[196:197], %[vb0] offset:8192\n"
    "ds_read_b64_tr_b16 a[198:199], %[vb0] offset:12288\n"
    "v_mfma_f32_16x16x32_bf16 a[180:183], a[200:203], v[88:91], a[180:183]\n"
    "s_add_u32 s46, s37, 0xc000\n"
    "ds_read_b64_tr_b16 a[200:201], %[vb1] offset:0\n"
    "ds_read_b64_tr_b16 a[202:203], %[vb1] offset:4096\n"
    "v_mfma_f32_16x16x32_bf16 a[180:183], a[204:207], v[112:115], a[180:183]\n"
    "s_waitcnt lgkmcnt(6)\n"
    "buffer_store_dwordx4 a[252:255], %[vos], %[sro], s47 offen offset:2048 sc0 sc1\n"
    "v_cvt_pk_bf16_f32 v223, v224, v225\n"
    "v_mfma_f32_16x16x32_bf16 v[124:127], a[208:211], v[72:75], v[124:127]\n"
    "s_add_u32 m0, s42, 0x18000\n"
    "ds_write_b64 %[stw], v[222:223] offset:192\n"
    "v_mfma_f32_16x16x32_bf16 v[124:127], a[212:215], v[96:99], v[124:127]\n"
    "buffer_load_dwordx4 %[vok], %[srk], s46 offen nt lds\n"
    "v_accvgpr_read_b32 v226, a28\n"
    "v_mfma_f32_16x16x32_bf16 v[188:191], a[208:211], v[80:83], v[188:191]\n"
    "v_accvgpr_read_b32 v227, a29\n"
    "v_accvgpr_read_b32 v228, a30\n"
    "v_mfma_f32_16x16x32_bf16 v[188:191], a[212:215], v[104:107], v[188:191]\n"
    "ds_read_b64_tr_b16 a[204:205], %[vb1] offset:8192\n"
    "ds_read_b64_tr_b16 a[206:207], %[vb1] offset:12288\n"
    "v_mfma_f32_16x16x32_bf16 a[184:187], a[208:211], v[88:91], a[184:187]\n"
    "s_add_u32 m0, s42, 0x18380\n"
    "ds_read_b64_tr_b16 a[208:209], %[vb2] offset:0\n"
    "ds_read_b64_tr_b16 a[210:211], %[vb2] offset:4096\n"
    "v_accvgpr_read_b32 v229, a31\n"
    "v_mfma_f32_16x16x32_bf16 a[184:187], a[212:215], v[112:115], a[184:187]\n"
    "v_mul_f32 v226, v219, v226\n"
    "v_mul_f32 v227, v219, v227\n"
    "v_mfma_f32_16x16x32_bf16 v[132:135], a[216:219], v[72:75], v[132:135]\n"
    "v_mul_f32 v228, v219, v228\n"
    "v_mul_f32 v229, v219, v229\n"
    "v_mfma_f32_16x16x32_bf16 v[132:135], a[220:223], v[96:99], v[132:135]\n"
    "s_cbranch_vccnz .Lpad33_%=\n"
    ".Lret33_%=:\n"
    "buffer_load_dwordx4 %[vok], %[srk], s46 offen offset:128 nt lds\n"
    "v_mfma_f32_16x16x32_bf16 v[196:199], a[216:219], v[80:83], v[196:199]\n"
    "v_cvt_pk_bf16_f32 v226, v226, v227\n"
    "v_cvt_pk_bf16_f32 v227, v228, v229\n"
    "v_mfma_f32_16x16x32_bf16 v[196:199], a[220:223], v[104:107], v[196:199]\n"
    "ds_write_b64 %[stw], v[226:227] offset:224\n"
    "v_mfma_f32_16x16x32_bf16 a[188:191], a[216:219], v[88:91], a[188:191]\n"
    "ds_read_b64_tr_b16 a[212:213], %[vb2] offset:8192\n"
    "ds_read_b64_tr_b16 a[214:215], %[vb2] offset:12288\n"
    "v_mfma_f32_16x16x32_bf16 a[188:191], a[220:223], v[112:115], a[188:191]\n"
    "s_waitcnt lgkmcnt(7)\n"
    "ds_read_b128 a[240:243], %[str] offset:0\n"
    "ds_read_b64_tr_b16 a[216:217], %[vb3] offset:0\n"
    "v_mfma_f32_16x16x32_bf16 v[76:79], a[192:195], v[120:123], v[76:79]\n"
    "ds_read_b64_tr_b16 a[218:219], %[vb3] offset:4096\n"
    "ds_read_b128 a[244:247], %[str] offset:1088\n"
    "v_mfma_f32_16x16x32_bf16 v[76:79], a[196:199], v[144:147], v[76:79]\n"
    "ds_read_b128 a[248:251], %[str] offset:2176\n"
    "ds_read_b128 a[252:255], %[str] offset:3264\n"
    "v_mfma_f32_16x16x32_bf16 v[140:143], a[192:195], v[128:131], v[140:143]\n"
    "v_accvgpr_read_b32 v222, a32\n"
    "v_accvgpr_read_b32 v223, a33\n"
    "v_mfma_f32_16x16x32_bf16 v[140:143], a[196:199], v[152:155], v[140:143]\n"
    "ds_read_b64_tr_b16 a[220:221], %[vb3] offset:8192\n"
    "ds_read_b64_tr_b16 a[222:223], %[vb3] offset:12288\n"
    "v_mfma_f32_16x16x32_bf16 v[204:207], a[192:195], v[136:139], v[204:207]\n"
    "s_waitcnt lgkmcnt(8)\n"
    "ds_read_b64_tr_b16 a[192:193], %[vb0] offset:1024\n"
    "v_mfma_f32_16x16x32_bf16 v[204:207], a[196:199], v[160:163], v[204:207]\n"
    "s_add_u32 m0, s42, 0x18700\n"
    "ds_read_b64_tr_b16 a[194:195], %[vb0] offset:5120\n"
    "v_accvgpr_read_b32 v224, a34\n"
    "v_mfma_f32_16x16x32_bf16 v[84:87], a[200:203], v[120:123], v[84:87]\n"
    "s_add_u32 s47, s40, 0x0\n"
    "v_accvgpr_read_b32 v225, a35\n"
    "v_mul_f32 v222, v220, v222\n"
    "v_mfma_f32_16x16x32_bf16 v[84:87], a[204:207], v[144:147], v[84:87]\n"
    "v_mul_f32 v223, v220, v223\n"
    "v_mul_f32 v224, v220, v224\n"
    "v_mfma_f32_16x16x32_bf16 v[148:151], a[200:203], v[128:131], v[148:151]\n"
    "buffer_load_dwordx4 %[vok], %[srk], s46 offen offset:256 nt lds\n"
    "v_mfma_f32_16x16x32_bf16 v[148:151], a[204:207], v[152:155], v[148:151]\n"
    "ds_read_b64_tr_b16 a[196:197], %[vb0] offset:9216\n"
    "ds_read_b64_tr_b16 a[198:199], %[vb0] offset:13312\n"
    "v_mfma_f32_16x16x32_bf16 v[212:215], a[200:203], v[136:139], v[212:215]\n"
    "ds_read_b64_tr_b16 a[200:201], %[vb1] offset:1024\n"
    "ds_read_b64_tr_b16 a[202:203], %[vb1] offset:5120\n"
    "v_mul_f32 v225, v220, v225\n"
    "v_cvt_pk_bf16_f32 v222, v222, v223\n"
    "v_mfma_f32_16x16x32_bf16 v[212:215], a[204:207], v[160:163], v[212:215]\n"
    "s_add_u32 m0, s42, 0x18a80\n"
    "v_cvt_pk_bf16_f32 v223, v224, v225\n"
    "v_mfma_f32_16x16x32_bf16 v[92:95], a[208:211], v[120:123], v[92:95]\n"
    "ds_write_b64 %[stw], v[222:223] offset:0\n"
    "v_mfma_f32_16x16x32_bf16 v[92:95], a[212:215], v[144:147], v[92:95]\n"
    "s_waitcnt lgkmcnt(7)\n"
    "buffer_store_dwordx4 a[240:243], %[vos], %[sro], s47 offen offset:0 sc0 sc1\n"
    "v_mfma_f32_16x16x32_bf16 v[156:159], a[208:211], v[128:131], v[156:159]\n"
    "buffer_load_dwordx4 %[vok], %[srk], s46 offen offset:384 nt lds\n"
    "v_accvgpr_read_b32 v226, a36\n"
    "v_mfma_f32_16x16x32_bf16 v[156:159], a[212:215], v[152:155], v[156:159]\n"
    "ds_read_b64_tr_b16 a[204:205], %[vb1] offset:9216\n"
    "ds_read_b64_tr_b16 a[206:207], %[vb1] offset:13312\n"
    "v_accvgpr_read_b32 v227, a37\n"
    "v_mfma_f32_16x16x32_bf16 a[168:171], a[208:211], v[136:139], a[168:171]\n"
    "ds_read_b64_tr_b16 a[208:209], %[vb2] offset:1024\n"
    "ds_read_b64_tr_b16 a[210:211], %[vb2] offset:5120\n"
    "v_accvgpr_read_b32 v228, a38\n"
    "v_mfma_f32_16x16x32_bf16 a[168:171], a[212:215], v[160:163], a[168:171]\n"
    "s_add_u32 s46, s37, 0x10000\n"
    "v_accvgpr_read_b32 v229, a39\n"
    "v_mul_f32 v226, v220, v226\n"
    "v_mfma_f32_16x16x32_bf16 v[100:103], a[216:219], v[120:123], v[100:103]\n"
    "s_add_u32 m0, s42, 0x10000\n"
    "v_mul_f32 v227, v220, v227\n"
    "v_mul_f32 v228, v220, v228\n"
    "v_mfma_f32_16x16x32_bf16 v[100:103], a[220:223], v[144:147], v[100:103]\n"
    "v_mul_f32 v229, v220, v229\n"
    "v_cvt_pk_bf16_f32 v226, v226, v227\n"
    "v_mfma_f32_16x16x32_bf16 v[164:167], a[216:219], v[128:131], v[164:167]\n"
    "buffer_load_dwordx4 %[vok], %[srk], s46 offen nt lds\n"
    "v_mfma_f32_16x16x32_bf16 v[164:167], a[220:223], v[152:155], v[164:167]\n"
    "ds_read_b64_tr_b16 a[212:213], %[vb2] offset:9216\n"
    "ds_read_b64_tr_b16 a[214:215], %[vb2] offset:13312\n"
    "v_mfma_f32_16x16x32_bf16 a[172:175], a[216:219], v[136:139], a[172:175]\n"
    "ds_read_b64_tr_b16 a[216:217], %[vb3] offset:1024\n"
    "ds_read_b64_tr_b16 a[218:219], %[vb3] offset:5120\n"
    "v_cvt_pk_bf16_f32 v227, v228, v229\n"
    "v_mfma_f32_16x16x32_bf16 a[172:175], a[220:223], v[160:163], a[172:175]\n"
    "s_waitcnt lgkmcnt(8)\n"
    "ds_write_b64 %[stw], v[226:227] offset:32\n"
    "v_mfma_f32_16x16x32_bf16 v[108:111], a[192:195], v[120:123], v[108:111]\n"
    "v_accvgpr_read_b32 v222, a40\n"
    "v_accvgpr_read_b32 v223, a41\n"
    "v_mfma_f32_16x16x32_bf16 v[108:111], a[196:199], v[144:147], v[108:111]\n"
    "v_accvgpr_read_b32 v224, a42\n"
    "v_accvgpr_read_b32 v225, a43\n"
    "v_mfma_f32_16x16x32_bf16 v[172:175], a[192:195], v[128:131], v[172:175]\n"
    "v_mul_f32 v222, v220, v222\n"
    "v_mul_f32 v223, v220, v223\n"
    "v_mfma_f32_16x16x32_bf16 v[172:175], a[196:199], v[152:155], v[172:175]\n"
    "s_cbranch_vccnz .Lpad34_%=\n"
    ".Lret34_%=:\n"
    "ds_read_b64_tr_b16 a[220:221], %[vb3] offset:9216\n"
    "ds_read_b64_tr_b16 a[222:223], %[vb3] offset:13312\n"
    "v_mfma_f32_16x16x32_bf16 a[176:179], a[192:195], v[136:139], a[176:179]\n"
    "s_waitcnt vmcnt(22)\n"
    "s_barrier\n"
    "buffer_store_dwordx4 a[244:247], %[vos], %[sro], s47 offen offset:2048 sc0 sc1\n"
    "v_mfma_f32_16x16x32_bf16 a[176:179], a[196:199], v[160:163], a[176:179]\n"
    "s_add_u32 m0, s42, 0x10380\n"
    "v_mul_f32 v224, v220, v224\n"
    "v_add_u32 %[vb0], 0x10000, %[vb0]\n"
    "v_mfma_f32_16x16x32_bf16 v[116:119], a[200:203], v[120:123], v[116:119]\n"
    "s_waitcnt lgkmcnt(5)\n"
    "v_add_u32 %[vb1], 0x10000, %[vb1]\n"
    "v_add_u32 %[vb2], 0x10000, %[vb2]\n"
    "v_add_u32 %[vb3], 0x10000, %[vb3]\n"
    "ds_read_b64_tr_b16 a[192:193], %[vb0] offset:49152\n"
    "ds_read_b64_tr_b16 a[194:195], %[vb0] offset:53248\n"
    "v_mfma_f32_16x16x32_bf16 v[116:119], a[204:207], v[144:147], v[116:119]\n"
    "buffer_load_dwordx4 %[vok], %[srk], s46 offen offset:128 nt lds\n"
    "v_mul_f32 v225, v220, v225\n"
    "v_mfma_f32_16x16x32_bf16 v[180:183], a[200:203], v[128:131], v[180:183]\n"
    "v_cvt_pk_bf16_f32 v222, v222, v223\n"
    "v_cvt_pk_bf16_f32 v223, v224, v225\n"
    "v_mfma_f32_16x16x32_bf16 v[180:183], a[204:207], v[152:155], v[180:183]\n"
    "ds_write_b64 %[stw], v[222:223] offset:64\n"
    "v_mfma_f32_16x16x32_bf16 a[180:183], a[200:203], v[136:139], a[180:183]\n"
    "ds_read_b64_tr_b16 a[196:197], %[vb0] offset:57344\n"
    "ds_read_b64_tr_b16 a[198:199], %[vb0] offset:61440\n"
    "v_mfma_f32_16x16x32_bf16 a[180:183], a[204:207], v[160:163], a[180:183]\n"
    "ds_read_b64_tr_b16 a[200:201], %[vb1] offset:49152\n"
    "ds_read_b64_tr_b16 a[202:203], %[vb1] offset:53248\n"
    "v_accvgpr_read_b32 v226, a44\n"
    "v_mfma_f32_16x16x32_bf16 v[124:127], a[208:211], v[120:123], v[124:127]\n"
    "v_accvgpr_read_b32 v227, a45\n"
    "v_accvgpr_read_b32 v228, a46\n"
    "v_mfma_f32_16x16x32_bf16 v[124:127], a[212:215], v[144:147], v[124:127]\n"
    "v_accvgpr_read_b32 v229, a47\n"
    "v_mul_f32 v226, v220, v226\n"
    "v_mfma_f32_16x16x32_bf16 v[188:191], a[208:211], v[128:131], v[188:191]\n"
    "v_mul_f32 v227, v220, v227\n"
    "v_mul_f32 v228, v220, v228\n"
    "v_mfma_f32_16x16x32_bf16 v[188:191], a[212:215], v[152:155], v[188:191]\n"
    "ds_read_b64_tr_b16 a[204:205], %[vb1] offset:57344\n"
    "ds_read_b64_tr_b16 a[206:207], %[vb1] offset:61440\n"
    "v_mfma_f32_16x16x32_bf16 a[184:187], a[208:211], v[136:139], a[184:187]\n"
    "ds_read_b64_tr_b16 a[208:209], %[vb2] offset:49152\n"
    "s_waitcnt lgkmcnt(8)\n"
    "ds_read_b64_tr_b16 a[210:211], %[vb2] offset:53248\n"
    "v_mfma_f32_16x16x32_bf16 a[184:187], a[212:215], v[160:163], a[184:187]\n"
    "s_add_u32 s47, s40, 0x1000\n"
    "v_mul_f32 v229, v220, v229\n"
    "v_cvt_pk_bf16_f32 v226, v226, v227\n"
    "v_mfma_f32_16x16x32_bf16 v[132:135], a[216:219], v[120:123], v[132:135]\n"
    "s_add_u32 m0, s42, 0x10700\n"
    "v_cvt_pk_bf16_f32 v227, v228, v229\n"
    "v_mfma_f32_16x16x32_bf16 v[132:135], a[220:223], v[144:147], v[132:135]\n"
    "ds_write_b64 %[stw], v[226:227] offset:96\n"
    "v_mfma_f32_16x16x32_bf16 v[196:199], a[216:219], v[128:131], v[196:199]\n"
    "v_accvgpr_read_b32 v222, a48\n"
    "v_accvgpr_read_b32 v223, a49\n"
    "v_mfma_f32_16x16x32_bf16 v[196:199], a[220:223], v[152:155], v[196:199]\n"
    "ds_read_b64_tr_b16 a[212:213], %[vb2] offset:57344\n"
    "ds_read_b64_tr_b16 a[214:215], %[vb2] offset:61440\n"
    "v_mfma_f32_16x16x32_bf16 a[188:191], a[216:219], v[136:139], a[188:191]\n"
    "buffer_load_dwordx4 %[vok], %[srk], s46 offen offset:256 nt lds\n"
    "v_mfma_f32_16x16x32_bf16 a[188:191], a[220:223], v[160:163], a[188:191]\n"
    "ds_read_b64_tr_b16 a[216:217], %[vb3] offset:49152\n"
    "ds_read_b64_tr_b16 a[218:219], %[vb3] offset:53248\n"
    "v_accvgpr_read_b32 v224, a50\n"
    "v_mfma_f32_16x16x32_bf16 v[76:79], a[192:195], v[168:171], v[76:79]\n"
    "s_waitcnt lgkmcnt(8)\n"
    "buffer_store_dwordx4 a[248:251], %[vos], %[sro], s47 offen offset:0 sc0 sc1\n"
    "v_accvgpr_read_b32 v225, a51\n"
    "v_mfma_f32_16x16x32_bf16 v[76:79], a[196:199], v[192:195], v[76:79]\n"
    "s_add_u32 m0, s42, 0x10a80\n"
    "v_mul_f32 v222, v220, v222\n"
    "v_mul_f32 v223, v220, v223\n"
    "v_mfma_f32_16x16x32_bf16 v[140:143], a[192:195], v[176:179], v[140:143]\n"
    "v_mul_f32 v224, v220, v224\n"
    "v_mul_f32 v225, v220, v225\n"
    "v_mfma_f32_16x16x32_bf16 v[140:143], a[196:199], v[200:203], v[140:143]\n"
    "ds_read_b64_tr_b16 a[220:221], %[vb3] offset:57344\n"
    "ds_read_b64_tr_b16 a[222:223], %[vb3] offset:61440\n"
    "v_mfma_f32_16x16x32_bf16 v[204:207], a[192:195], v[184:187], v[204:207]\n"
    "buffer_load_dwordx4 %[vok], %[srk], s46 offen offset:384 nt lds\n"
    "v_mfma_f32_16x16x32_bf16 v[204:207], a[196:199], v[208:211], v[204:207]\n"
    "ds_read_b64_tr_b16 a[192:193], %[vb0] offset:50176\n"
    "ds_read_b64_tr_b16 a[194:195], %[vb0] offset:54272\n"
    "v_cvt_pk_bf16_f32 v222, v222, v223\n"
    "v_mfma_f32_16x16x32_bf16 v[84:87], a[200:203], v[168:171], v[84:87]\n"
    "s_waitcnt lgkmcnt(6)\n"
    "v_cvt_pk_bf16_f32 v223, v224, v225\n"
    "v_mfma_f32_16x16x32_bf16 v[84:87], a[204:207], v[192:195], v[84:87]\n"
    "ds_write_b64 %[stw], v[222:223] offset:128\n"
    "v_mfma_f32_16x16x32_bf16 v[148:151], a[200:203], v[176:179], v[148:151]\n"
    "v_accvgpr_read_b32 v226, a52\n"
    "v_accvgpr_read_b32 v227, a53\n"
    "v_mfma_f32_16x16x32_bf16 v[148:151], a[204:207], v[200:203], v[148:151]\n"
    "ds_read_b64_tr_b16 a[196:197], %[vb0] offset:58368\n"
    "ds_read_b64_tr_b16 a[198:199], %[vb0] offset:62464\n"
    "v_mfma_f32_16x16x32_bf16 v[212:215], a[200:203], v[184:187], v[212:215]\n"
    "ds_read_b64_tr_b16 a[200:201], %[vb1] offset:50176\n"
    "ds_read_b64_tr_b16 a[202:203], %[vb1] offset:54272\n"
    "v_accvgpr_read_b32 v228, a54\n"
    "v_mfma_f32_16x16x32_bf16 v[212:215], a[204:207], v[208:211], v[212:215]\n"
    "s_cbranch_vccnz .Lpad35_%=\n"
    ".Lret35_%=:\n"
    "v_accvgpr_read_b32 v229, a55\n"
    "v_mul_f32 v226, v220, v226\n"
    "v_mfma_f32_16x16x32_bf16 v[92:95], a[208:211], v[168:171], v[92:95]\n"
    "v_mul_f32 v227, v220, v227\n"
    "v_mul_f32 v228, v220, v228\n"
    "v_mfma_f32_16x16x32_bf16 v[92:95], a[212:215], v[192:195], v[92:95]\n"
    "v_mul_f32 v229, v220, v229\n"
    "v_cvt_pk_bf16_f32 v226, v226, v227\n"
    "v_mfma_f32_16x16x32_bf16 v[156:159], a[208:211], v[176:179], v[156:159]\n"
    "buffer_store_dwordx4 a[252:255], %[vos], %[sro], s47 offen offset:2048 sc0 sc1\n"
    "v_mfma_f32_16x16x32_bf16 v[156:159], a[212:215], v[200:203], v[156:159]\n"
    "s_add_u32 s46, s37, 0x14000\n"
    "ds_read_b64_tr_b16 a[204:205], %[vb1] offset:58368\n"
    "ds_read_b64_tr_b16 a[206:207], %[vb1] offset:62464\n"
    "v_mfma_f32_16x16x32_bf16 a[168:171], a[208:211], v[184:187], a[168:171]\n"
    "s_add_u32 m0, s42, 0x14000\n"
    "ds_read_b64_tr_b16 a[208:209], %[vb2] offset:50176\n"
    "ds_read_b64_tr_b16 a[210:211], %[vb2] offset:54272\n"
    "v_cvt_pk_bf16_f32 v227, v228, v229\n"
    "v_mfma_f32_16x16x32_bf16 a[168:171], a[212:215], v[208:211], a[168:171]\n"
    "s_waitcnt lgkmcnt(8)\n"
    "ds_write_b64 %[stw], v[226:227] offset:160\n"
    "v_mfma_f32_16x16x32_bf16 v[100:103], a[216:219], v[168:171], v[100:103]\n"
    "buffer_load_dwordx4 %[vok], %[srk], s46 offen nt lds\n"
    "v_mfma_f32_16x16x32_bf16 v[100:103], a[220:223], v[192:195], v[100:103]\n"
    "v_accvgpr_read_b32 v222, a56\n"
    "v_accvgpr_read_b32 v223, a57\n"
    "v_mfma_f32_16x16x32_bf16 v[164:167], a[216:219], v[176:179], v[164:167]\n"
    "v_accvgpr_read_b32 v224, a58\n"
    "v_accvgpr_read_b32 v225, a59\n"
    "v_mfma_f32_16x16x32_bf16 v[164:167], a[220:223], v[200:203], v[164:167]\n"
    "ds_read_b64_tr_b16 a[212:213], %[vb2] offset:58368\n"
    "ds_read_b64_tr_b16 a[214:215], %[vb2] offset:62464\n"
    "v_mfma_f32_16x16x32_bf16 a[172:175], a[216:219], v[184:187], a[172:175]\n"
    "ds_read_b64_tr_b16 a[216:217], %[vb3] offset:50176\n"
    "ds_read_b64_tr_b16 a[218:219], %[vb3] offset:54272\n"
    "v_mul_f32 v222, v220, v222\n"
    "v_mfma_f32_16x16x32_bf16 a[172:175], a[220:223], v[208:211], a[172:175]\n"
    "v_mul_f32 v223, v220, v223\n"
    "v_mul_f32 v224, v220, v224\n"
    "v_mfma_f32_16x16x32_bf16 v[108:111], a[192:195], v[168:171], v[108:111]\n"
    "s_waitcnt lgkmcnt(8)\n"
    "v_mul_f32 v225, v220, v225\n"
    "v_cvt_pk_bf16_f32 v222, v222, v223\n"
    "v_mfma_f32_16x16x32_bf16 v[108:111], a[196:199], v[192:195], v[108:111]\n"
    "v_cvt_pk_bf16_f32 v223, v224, v225\n"
    "v_mfma_f32_16x16x32_bf16 v[172:175], a[192:195], v[176:179], v[172:175]\n"
    "ds_write_b64 %[stw], v[222:223] offset:192\n"
    "v_mfma_f32_16x16x32_bf16 v[172:175], a[196:199], v[200:203], v[172:175]\n"
    "ds_read_b64_tr_b16 a[220:221], %[vb3] offset:58368\n"
    "ds_read_b64_tr_b16 a[222:223], %[vb3] offset:62464\n"
    "v_mfma_f32_16x16x32_bf16 a[176:179], a[192:195], v[184:187], a[176:179]\n"
    "s_waitcnt vmcnt(25)\n"
    "s_barrier\n"
    "v_accvgpr_read_b32 v226, a60\n"
    "v_accvgpr_read_b32 v227, a61\n"
    "v_mfma_f32_16x16x32_bf16 a[176:179], a[196:199], v[208:211], a[176:179]\n"
    "s_add_u32 m0, s42, 0x14380\n"
    "v_add_u32 %[kb0], 0xffff0000, %[kb0]\n"
    "v_add_u32 %[kb1], 0xffff0000, %[kb1]\n"
    "v_mfma_f32_16x16x32_bf16 v[116:119], a[200:203], v[168:171], v[116:119]\n"
    "s_waitcnt lgkmcnt(5)\n"
    "ds_read_b128 a[192:195], %[kb0] offset:16384\n"
    "v_accvgpr_read_b32 v228, a62\n"
    "v_mfma_f32_16x16x32_bf16 v[116:119], a[204:207], v[192:195], v[116:119]\n"
    "v_accvgpr_read_b32 v229, a63\n"
    "v_mul_f32 v226, v220, v226\n"
    "v_mfma_f32_16x16x32_bf16 v[180:183], a[200:203], v[176:179], v[180:183]\n"
    "buffer_load_dwordx4 %[vok], %[srk], s46 offen offset:128 nt lds\n"
    "v_mfma_f32_16x16x32_bf16 v[180:183], a[204:207], v[200:203], v[180:183]\n"
    "ds_read_b128 a[196:199], %[kb1] offset:16384\n"
    "v_mul_f32 v227, v220, v227\n"
    "v_mfma_f32_16x16x32_bf16 a[180:183], a[200:203], v[184:187], a[180:183]\n"
    "ds_read_b128 a[200:203], %[kb0] offset:17408\n"
    "v_mul_f32 v228, v220, v228\n"
    "v_mfma_f32_16x16x32_bf16 a[180:183], a[204:207], v[208:211], a[180:183]\n"
    "s_add_u32 m0, s42, 0x14700\n"
    "v_mul_f32 v229, v220, v229\n"
    "v_cvt_pk_bf16_f32 v226, v226, v227\n"
    "v_mfma_f32_16x16x32_bf16 v[124:127], a[208:211], v[168:171], v[124:127]\n"
    "v_cvt_pk_bf16_f32 v227, v228, v229\n"
    "v_mfma_f32_16x16x32_bf16 v[124:127], a[212:215], v[192:195], v[124:127]\n"
    "ds_write_b64 %[stw], v[226:227] offset:224\n"
    "v_mfma_f32_16x16x32_bf16 v[188:191], a[208:211], v[176:179], v[188:191]\n"
    "ds_read_b128 a[240:243], %[str] offset:0\n"
    "v_mfma_f32_16x16x32_bf16 v[188:191], a[212:215], v[200:203], v[188:191]\n"
    "buffer_load_dwordx4 %[vok], %[srk], s46 offen offset:256 nt lds\n"
    "v_mfma_f32_16x16x32_bf16 a[184:187], a[208:211], v[184:187], a[184:187]\n"
    "ds_read_b128 a[244:247], %[str] offset:1088\n"
    "ds_read_b128 a[204:207], %[kb1] offset:17408\n"
    "v_mfma_f32_16x16x32_bf16 a[184:187], a[212:215], v[208:211], a[184:187]\n"
    "s_waitcnt lgkmcnt(6)\n"
    "ds_read_b128 a[248:251], %[str] offset:2176\n"
    "ds_read_b128 a[208:211], %[kb0] offset:18432\n"
    "v_mfma_f32_16x16x32_bf16 v[132:135], a[216:219], v[168:171], v[132:135]\n"
    "s_add_u32 m0, s42, 0x14a80\n"
    "ds_read_b128 a[252:255], %[str] offset:3264\n"
    "v_mfma_f32_16x16x32_bf16 v[132:135], a[220:223], v[192:195], v[132:135]\n"
    "s_cbranch_vccnz .Lpad36_%=\n"
    ".Lret36_%=:\n"
    "v_mfma_f32_16x16x32_bf16 v[196:199], a[216:219], v[176:179], v[196:199]\n"
    "buffer_load_dwordx4 %[vok], %[srk], s46 offen offset:384 nt lds\n"
    "v_mfma_f32_16x16x32_bf16 v[196:199], a[220:223], v[200:203], v[196:199]\n"
    "v_mfma_f32_16x16x32_bf16 a[188:191], a[216:219], v[184:187], a[188:191]\n"
    "v_mfma_f32_16x16x32_bf16 a[188:191], a[220:223], v[208:211], a[188:191]\n"
    "s_waitcnt lgkmcnt(0)\n"
    "s_mov_b32 s41, s40\n"
    "s_mov_b32 s40, s39\n"
    "s_mov_b32 s36, s37\n"
    "s_add_u32 s38, s38, 0x1800000\n"
    "s_add_u32 s37, s37, 0x1800000\n"
    "s_add_u32 s39, s39, 0x1800000\n"
    "s_add_u32 s45, s45, 1\n"
    "s_cmp_ge_u32 s45, 3\n"
    "s_cselect_b32 s37, 0xf0000000, s37\n"
    "s_cselect_b32 s39, 0xf0000000, s39\n"
    "s_cmp_lt_u32 s45, 4\n"
    "s_cbranch_scc1 1b\n"
    "s_nop 7\n"
    "s_add_u32 s47, s41, 0x2000\n"
    "buffer_store_dwordx4 a[240:243], %[vos], %[sro], s47 offen offset:0 sc0 sc1\n"
    "buffer_store_dwordx4 a[244:247], %[vos], %[sro], s47 offen offset:2048 sc0 sc1\n"
    "s_add_u32 s47, s41, 0x3000\n"
    "buffer_store_dwordx4 a[248:251], %[vos], %[sro], s47 offen offset:0 sc0 sc1\n"
    "buffer_store_dwordx4 a[252:255], %[vos], %[sro], s47 offen offset:2048 sc0 sc1\n"
    "v_pk_mul_f32 v[76:77], v[76:77], v[218:219] op_sel:[0,1] op_sel_hi:[1,1]\n"
    "v_pk_mul_f32 v[78:79], v[78:79], v[218:219] op_sel:[0,1] op_sel_hi:[1,1]\n"
    "v_cvt_pk_bf16_f32 v76, v76, v77\n"
    "v_cvt_pk_bf16_f32 v77, v78, v79\n"
    "v_pk_mul_f32 v[84:85], v[84:85], v[218:219] op_sel:[0,1] op_sel_hi:[1,1]\n"
    "v_pk_mul_f32 v[86:87], v[86:87], v[218:219] op_sel:[0,1] op_sel_hi:[1,1]\n"
    "v_cvt_pk_bf16_f32 v84, v84, v85\n"
    "v_cvt_pk_bf16_f32 v85, v86, v87\n"
    "v_pk_mul_f32 v[92:93], v[92:93], v[218:219] op_sel:[0,1] op_sel_hi:[1,1]\n"
    "v_pk_mul_f32 v[94:95], v[94:95], v[218:219] op_sel:[0,1] op_sel_hi:[1,1]\n"
    "v_cvt_pk_bf16_f32 v92, v92, v93\n"
    "v_cvt_pk_bf16_f32 v93, v94, v95\n"
    "v_pk_mul_f32 v[100:101], v[100:101], v[218:219] op_sel:[0,1] op_sel_hi:[1,1]\n"
    "v_pk_mul_f32 v[102:103], v[102:103], v[218:219] op_sel:[0,1] op_sel_hi:[1,1]\n"
    "v_cvt_pk_bf16_f32 v100, v100, v101\n"
    "v_cvt_pk_bf16_f32 v101, v102, v103\n"
    "v_pk_mul_f32 v[108:109], v[108:109], v[218:219] op_sel:[0,1] op_sel_hi:[1,1]\n"
    "v_pk_mul_f32 v[110:111], v[110:111], v[218:219] op_sel:[0,1] op_sel_hi:[1,1]\n"
    "v_cvt_pk_bf16_f32 v108, v108, v109\n"
    "v_cvt_pk_bf16_f32 v109, v110, v111\n"
    "v_pk_mul_f32 v[116:117], v[116:117], v[218:219] op_sel:[0,1] op_sel_hi:[1,1]\n"
    "v_pk_mul_f32 v[118:119], v[118:119], v[218:219] op_sel:[0,1] op_sel_hi:[1,1]\n"
    "v_cvt_pk_bf16_f32 v116, v116, v117\n"
    "v_cvt_pk_bf16_f32 v117, v118, v119\n"
    "v_pk_mul_f32 v[124:125], v[124:125], v[218:219] op_sel:[0,1] op_sel_hi:[1,1]\n"
    "v_pk_mul_f32 v[126:127], v[126:127], v[218:219] op_sel:[0,1] op_sel_hi:[1,1]\n"
    "v_cvt_pk_bf16_f32 v124, v124, v125\n"
    "v_cvt_pk_bf16_f32 v125, v126, v127\n"
    "v_pk_mul_f32 v[132:133], v[132:133], v[218:219] op_sel:[0,1] op_sel_hi:[1,1]\n"
    "v_pk_mul_f32 v[134:135], v[134:135], v[218:219] op_sel:[0,1] op_sel_hi:[1,1]\n"
    "v_cvt_pk_bf16_f32 v132, v132, v133\n"
    "v_cvt_pk_bf16_f32 v133, v134, v135\n"
    "ds_write_b64 %[stw], v[76:77] offset:0\n"
    "ds_write_b64 %[stw], v[84:85] offset:32\n"
    "ds_write_b64 %[stw], v[92:93] offset:64\n"
    "ds_write_b64 %[stw], v[100:101] offset:96\n"
    "ds_write_b64 %[stw], v[108:109] offset:128\n"
    "ds_write_b64 %[stw], v[116:117] offset:160\n"
    "ds_write_b64 %[stw], v[124:125] offset:192\n"
    "ds_write_b64 %[stw], v[132:133] offset:224\n"
    "ds_read_b128 a[0:3], %[str] offset:0\n"
    "ds_read_b128 a[4:7], %[str] offset:1088\n"
    "ds_read_b128 a[8:11], %[str] offset:2176\n"
    "ds_read_b128 a[12:15], %[str] offset:3264\n"
    "v_pk_mul_f32 v[140:141], v[140:141], v[220:221] op_sel:[0,0] op_sel_hi:[1,0]\n"
    "v_pk_mul_f32 v[142:143], v[142:143], v[220:221] op_sel:[0,0] op_sel_hi:[1,0]\n"
    "v_cvt_pk_bf16_f32 v140, v140, v141\n"
    "v_cvt_pk_bf16_f32 v141, v142, v143\n"
    "v_pk_mul_f32 v[148:149], v[148:149], v[220:221] op_sel:[0,0] op_sel_hi:[1,0]\n"
    "v_pk_mul_f32 v[150:151], v[150:151], v[220:221] op_sel:[0,0] op_sel_hi:[1,0]\n"
    "v_cvt_pk_bf16_f32 v148, v148, v149\n"
    "v_cvt_pk_bf16_f32 v149, v150, v151\n"
    "v_pk_mul_f32 v[156:157], v[156:157], v[220:221] op_sel:[0,0] op_sel_hi:[1,0]\n"
    "v_pk_mul_f32 v[158:159], v[158:159], v[220:221] op_sel:[0,0] op_sel_hi:[1,0]\n"
    "v_cvt_pk_bf16_f32 v156, v156, v157\n"
    "v_cvt_pk_bf16_f32 v157, v158, v159\n"
    "v_pk_mul_f32 v[164:165], v[164:165], v[220:221] op_sel:[0,0] op_sel_hi:[1,0]\n"
    "v_pk_mul_f32 v[166:167], v[166:167], v[220:221] op_sel:[0,0] op_sel_hi:[1,0]\n"
    "v_cvt_pk_bf16_f32 v164, v164, v165\n"
    "v_cvt_pk_bf16_f32 v165, v166, v167\n"
    "v_pk_mul_f32 v[172:173], v[172:173], v[220:221] op_sel:[0,0] op_sel_hi:[1,0]\n"
    "v_pk_mul_f32 v[174:175], v[174:175], v[220:221] op_sel:[0,0] op_sel_hi:[1,0]\n"
    "v_cvt_pk_bf16_f32 v172, v172, v173\n"
    "v_cvt_pk_bf16_f32 v173, v174, v175\n"
    "v_pk_mul_f32 v[180:181], v[180:181], v[220:221] op_sel:[0,0] op_sel_hi:[1,0]\n"
    "v_pk_mul_f32 v[182:183], v[182:183], v[220:221] op_sel:[0,0] op_sel_hi:[1,0]\n"
    "v_cvt_pk_bf16_f32 v180, v180, v181\n"
    "v_cvt_pk_bf16_f32 v181, v182, v183\n"
    "v_pk_mul_f32 v[188:189], v[188:189], v[220:221] op_sel:[0,0] op_sel_hi:[1,0]\n"
    "v_pk_mul_f32 v[190:191], v[190:191], v[220:221] op_sel:[0,0] op_sel_hi:[1,0]\n"
    "v_cvt_pk_bf16_f32 v188, v188, v189\n"
    "v_cvt_pk_bf16_f32 v189, v190, v191\n"
    "v_pk_mul_f32 v[196:197], v[196:197], v[220:221] op_sel:[0,0] op_sel_hi:[1,0]\n"
    "v_pk_mul_f32 v[198:199], v[198:199], v[220:221] op_sel:[0,0] op_sel_hi:[1,0]\n"
    "v_cvt_pk_bf16_f32 v196, v196, v197\n"
    "v_cvt_pk_bf16_f32 v197, v198, v199\n"
    "s_add_u32 s47, s41, 0x0\n"
    "s_waitcnt lgkmcnt(3)\n"
    "buffer_store_dwordx4 a[0:3], %[vos], %[sro], s47 offen offset:256 sc0 sc1\n"
    "s_waitcnt lgkmcnt(1)\n"
    "buffer_store_dwordx4 a[4:7], %[vos], %[sro], s47 offen offset:2304 sc0 sc1\n"
    "s_add_u32 s47, s41, 0x1000\n"
    "buffer_store_dwordx4 a[8:11], %[vos], %[sro], s47 offen offset:256 sc0 sc1\n"
    "s_waitcnt lgkmcnt(0)\n"
    "buffer_store_dwordx4 a[12:15], %[vos], %[sro], s47 offen offset:2304 sc0 sc1\n"
    "ds_write_b64 %[stw], v[140:141] offset:0\n"
    "ds_write_b64 %[stw], v[148:149] offset:32\n"
    "ds_write_b64 %[stw], v[156:157] offset:64\n"
    "ds_write_b64 %[stw], v[164:165] offset:96\n"
    "ds_write_b64 %[stw], v[172:173] offset:128\n"
    "ds_write_b64 %[stw], v[180:181] offset:160\n"
    "ds_write_b64 %[stw], v[188:189] offset:192\n"
    "ds_write_b64 %[stw], v[196:197] offset:224\n"
    "ds_read_b128 a[16:19], %[str] offset:0\n"
    "ds_read_b128 a[20:23], %[str] offset:1088\n"
    "ds_read_b128 a[24:27], %[str] offset:2176\n"
    "ds_read_b128 a[28:31], %[str] offset:3264\n"
    "v_pk_mul_f32 v[204:205], v[204:205], v[220:221] op_sel:[0,1] op_sel_hi:[1,1]\n"
    "v_pk_mul_f32 v[206:207], v[206:207], v[220:221] op_sel:[0,1] op_sel_hi:[1,1]\n"
    "v_cvt_pk_bf16_f32 v204, v204, v205\n"
    "v_cvt_pk_bf16_f32 v205, v206, v207\n"
    "v_pk_mul_f32 v[212:213], v[212:213], v[220:221] op_sel:[0,1] op_sel_hi:[1,1]\n"
    "v_pk_mul_f32 v[214:215], v[214:215], v[220:221] op_sel:[0,1] op_sel_hi:[1,1]\n"
    "v_cvt_pk_bf16_f32 v212, v212, v213\n"
    "v_cvt_pk_bf16_f32 v213, v214, v215\n"
    "v_accvgpr_read_b32 v8, a168\n"
    "v_accvgpr_read_b32 v9, a169\n"
    "v_accvgpr_read_b32 v10, a170\n"
    "v_accvgpr_read_b32 v11, a171\n"
    "v_pk_mul_f32 v[8:9], v[8:9], v[220:221] op_sel:[0,1] op_sel_hi:[1,1]\n"
    "v_pk_mul_f32 v[10:11], v[10:11], v[220:221] op_sel:[0,1] op_sel_hi:[1,1]\n"
    "v_cvt_pk_bf16_f32 v8, v8, v9\n"
    "v_cvt_pk_bf16_f32 v9, v10, v11\n"
    "v_accvgpr_read_b32 v12, a172\n"
    "v_accvgpr_read_b32 v13, a173\n"
    "v_accvgpr_read_b32 v14, a174\n"
    "v_accvgpr_read_b32 v15, a175\n"
    "v_pk_mul_f32 v[12:13], v[12:13], v[220:221] op_sel:[0,1] op_sel_hi:[1,1]\n"
    "v_pk_mul_f32 v[14:15], v[14:15], v[220:221] op_sel:[0,1] op_sel_hi:[1,1]\n"
    "v_cvt_pk_bf16_f32 v12, v12, v13\n"
    "v_cvt_pk_bf16_f32 v13, v14, v15\n"
    "v_accvgpr_read_b32 v16, a176\n"
    "v_accvgpr_read_b32 v17, a177\n"
    "v_accvgpr_read_b32 v18, a178\n"
    "v_accvgpr_read_b32 v19, a179\n"
    "v_pk_mul_f32 v[16:17], v[16:17], v[220:221] op_sel:[0,1] op_sel_hi:[1,1]\n"
    "v_pk_mul_f32 v[18:19], v[18:19], v[220:221] op_sel:[0,1] op_sel_hi:[1,1]\n"
    "v_cvt_pk_bf16_f32 v16, v16, v17\n"
    "v_cvt_pk_bf16_f32 v17, v18, v19\n"
    "v_accvgpr_read_b32 v20, a180\n"
    "v_accvgpr_read_b32 v21, a181\n"
    "v_accvgpr_read_b32 v22, a182\n"
    "v_accvgpr_read_b32 v23, a183\n"
    "v_pk_mul_f32 v[20:21], v[20:21], v[220:221] op_sel:[0,1] op_sel_hi:[1,1]\n"
    "v_pk_mul_f32 v[22:23], v[22:23], v[220:221] op_sel:[0,1] op_sel_hi:[1,1]\n"
    "v_cvt_pk_bf16_f32 v20, v20, v21\n"
    "v_cvt_pk_bf16_f32 v21, v22, v23\n"
    "v_accvgpr_read_b32 v24, a184\n"
    "v_accvgpr_read_b32 v25, a185\n"
    "v_accvgpr_read_b32 v26, a186\n"
    "v_accvgpr_read_b32 v27, a187\n"
    "v_pk_mul_f32 v[24:25], v[24:25], v[220:221] op_sel:[0,1] op_sel_hi:[1,1]\n"
    "v_pk_mul_f32 v[26:27], v[26:27], v[220:221] op_sel:[0,1] op_sel_hi:[1,1]\n"
    "v_cvt_pk_bf16_f32 v24, v24, v25\n"
    "v_cvt_pk_bf16_f32 v25, v26, v27\n"
    "v_accvgpr_read_b32 v28, a188\n"
    "v_accvgpr_read_b32 v29, a189\n"
    "v_accvgpr_read_b32 v30, a190\n"
    "v_accvgpr_read_b32 v31, a191\n"
    "v_pk_mul_f32 v[28:29], v[28:29], v[220:221] op_sel:[0,1] op_sel_hi:[1,1]\n"
    "v_pk_mul_f32 v[30:31], v[30:31], v[220:221] op_sel:[0,1] op_sel_hi:[1,1]\n"
    "v_cvt_pk_bf16_f32 v28, v28, v29\n"
    "v_cvt_pk_bf16_f32 v29, v30, v31\n"
    "s_add_u32 s47, s41, 0x2000\n"
    "s_waitcnt lgkmcnt(0)\n"
    "buffer_store_dwordx4 a[16:19], %[vos], %[sro], s47 offen offset:256 sc0 sc1\n"
    "buffer_store_dwordx4 a[20:23], %[vos], %[sro], s47 offen offset:2304 sc0 sc1\n"
    "s_add_u32 s47, s41, 0x3000\n"
    "buffer_store_dwordx4 a[24:27], %[vos], %[sro], s47 offen offset:256 sc0 sc1\n"
    "buffer_store_dwordx4 a[28:31], %[vos], %[sro], s47 offen offset:2304 sc0 sc1\n"
    "ds_write_b64 %[stw], v[204:205] offset:0\n"
    "ds_write_b64 %[stw], v[212:213] offset:32\n"
    "ds_write_b64 %[stw], v[8:9] offset:64\n"
    "ds_write_b64 %[stw], v[12:13] offset:96\n"
    "ds_write_b64 %[stw], v[16:17] offset:128\n"
    "ds_write_b64 %[stw], v[20:21] offset:160\n"
    "ds_write_b64 %[stw], v[24:25] offset:192\n"
    "ds_write_b64 %[stw], v[28:29] offset:224\n"
    "ds_read_b128 a[32:35], %[str] offset:0\n"
    "ds_read_b128 a[36:39], %[str] offset:1088\n"
    "ds_read_b128 a[40:43], %[str] offset:2176\n"
    "ds_read_b128 a[44:47], %[str] offset:3264\n"
    "s_add_u32 s47, s41, 0x4000\n"
    "s_waitcnt lgkmcnt(3)\n"
    "buffer_store_dwordx4 a[32:35], %[vos], %[sro], s47 offen offset:256 sc0 sc1\n"
    "s_waitcnt lgkmcnt(2)\n"
    "buffer_store_dwordx4 a[36:39], %[vos], %[sro], s47 offen offset:2304 sc0 sc1\n"
    "s_add_u32 s47, s41, 0x5000\n"
    "s_waitcnt lgkmcnt(1)\n"
    "buffer_store_dwordx4 a[40:43], %[vos], %[sro], s47 offen offset:256 sc0 sc1\n"
    "s_waitcnt lgkmcnt(0)\n"
    "buffer_store_dwordx4 a[44:47], %[vos], %[sro], s47 offen offset:2304 sc0 sc1\n"
    "s_waitcnt vmcnt(0)\n"
    "s_branch 9f\n"
    ".Lpad0_%=:\n"
    "s_sleep 1\n"
    "s_branch .Lret0_%=\n"
    ".Lpad1_%=:\n"
    "s_sleep 1\n"
    "s_branch .Lret1_%=\n"
    ".Lpad2_%=:\n"
    "s_sleep 1\n"
    "s_branch .Lret2_%=\n"
    ".Lpad3_%=:\n"
    "s_sleep 1\n"
    "s_branch .Lret3_%=\n"
    ".Lpad4_%=:\n"
    "s_sleep 1\n"
    "s_branch .Lret4_%=\n"
    ".Lpad5_%=:\n"
    "s_sleep 1\n"
    "s_branch .Lret5_%=\n"
    ".Lpad6_%=:\n"
    "s_sleep 1\n"
    "s_branch .Lret6_%=\n"
    ".Lpad7_%=:\n"
    "s_sleep 1\n"
    "s_branch .Lret7_%=\n"
    ".Lpad8_%=:\n"
    "s_sleep 1\n"
    "s_branch .Lret8_%=\n"
    ".Lpad9_%=:\n"
    "s_sleep 1\n"
    "s_branch .Lret9_%=\n"
    ".Lpad10_%=:\n"
    "s_sleep 1\n"
    "s_branch .Lret10_%=\n"
    ".Lpad11_%=:\n"
    "s_sleep 1\n"
    "s_branch .Lret11_%=\n"
    ".Lpad12_%=:\n"
    "s_sleep 1\n"
    "s_branch .Lret12_%=\n"
    ".Lpad13_%=:\n"
    "s_sleep 1\n"
    "s_branch .Lret13_%=\n"
    ".Lpad14_%=:\n"
    "s_sleep 1\n"
    "s_branch .Lret14_%=\n"
    ".Lpad15_%=:\n"
    "s_sleep 1\n"
    "s_branch .Lret15_%=\n"
    ".Lpad16_%=:\n"
    "s_sleep 1\n"
    "s_branch .Lret16_%=\n"
    ".Lpad17_%=:\n"
    "s_sleep 1\n"
    "s_branch .Lret17_%=\n"
    ".Lpad18_%=:\n"
    "s_sleep 1\n"
    "s_branch .Lret18_%=\n"
    ".Lpad19_%=:\n"
    "s_sleep 1\n"
    "s_branch .Lret19_%=\n"
    ".Lpad20_%=:\n"
    "s_sleep 1\n"
    "s_branch .Lret20_%=\n"
    ".Lpad21_%=:\n"
    "s_sleep 1\n"
    "s_branch .Lret21_%=\n"
    ".Lpad22_%=:\n"
    "s_sleep 1\n"
    "s_branch .Lret22_%=\n"
    ".Lpad23_%=:\n"
    "s_sleep 1\n"
    "s_branch .Lret23_%=\n"
    ".Lpad24_%=:\n"
    "s_sleep 1\n"
    "s_branch .Lret24_%=\n"
    ".Lpad25_%=:\n"
    "s_sleep 1\n"
    "s_branch .Lret25_%=\n"
    ".Lpad26_%=:\n"
    "s_sleep 1\n"
    "s_branch .Lret26_%=\n"
    ".Lpad27_%=:\n"
    "s_sleep 1\n"
    "s_branch .Lret27_%=\n"
    ".Lpad28_%=:\n"
    "s_sleep 1\n"
    "s_branch .Lret28_%=\n"
    ".Lpad29_%=:\n"
    "s_sleep 1\n"
    "s_branch .Lret29_%=\n"
    ".Lpad30_%=:\n"
    "s_sleep 1\n"
    "s_branch .Lret30_%=\n"
    ".Lpad31_%=:\n"
    "s_sleep 1\n"
    "s_branch .Lret31_%=\n"
    ".Lpad32_%=:\n"
    "s_sleep 1\n"
    "s_branch .Lret32_%=\n"
    ".Lpad33_%=:\n"
    "s_sleep 1\n"
    "s_branch .Lret33_%=\n"
    ".Lpad34_%=:\n"
    "s_sleep 1\n"
    "s_branch .Lret34_%=\n"
    ".Lpad35_%=:\n"
    "s_sleep 1\n"
    "s_branch .Lret35_%=\n"
    ".Lpad36_%=:\n"
    "s_sleep 1\n"
    "s_branch .Lret36_%=\n"
    "9:\n"
    : [kb0] "+v"(kb0), [kb1] "+v"(kb1), [qb0] "+v"(qb0), [qb1] "+v"(qb1), [vb0] "+v"(vb0), [vb1] "+v"(vb1), [vb2] "+v"(vb2), [vb3] "+v"(vb3)
    : [voe] "v"(voe), [voo] "v"(voo), [vok] "v"(vok), [vov] "v"(vov), [stw] "v"(stw), [str] "v"(str), [vos] "v"(vos),
      [srq] "s"(srq), [srk] "s"(srk), [srv] "s"(srv), [sro] "s"(sro), [hk] "s"(hk), [hv] "s"(hv), [q0] "s"(q0), [sw] "s"(sw), [wv] "s"(w), [sk] "s"(((j >> 1) % 2u) * 10u + 0u)
    : "v0", "v1", "v2", "v3", "v4", "v5", "v6", "v7", "v8", "v9", "v10", "v11", "v12", "v13", "v14", "v15", "v16", "v17", "v18", "v19", "v20", "v21", "v22", "v23", "v24", "v25", "v26", "v27", "v28", "v29", "v30", "v31", "v32", "v33", "v34", "v35", "v36", "v37", "v38", "v39", "v40", "v41", "v42", "v43", "v44", "v45", "v46", "v47", "v48", "v49", "v50", "v51", "v52", "v53", "v54", "v55", "v56", "v57", "v58", "v59", "v60", "v61", "v62", "v63", "v64", "v65", "v66", "v67", "v68", "v69", "v70", "v71", "v72", "v73", "v74", "v75", "v76", "v77", "v78", "v79", "v80", "v81", "v82", "v83", "v84", "v85", "v86", "v87", "v88", "v89", "v90", "v91", "v92", "v93", "v94", "v95", "v96", "v97", "v98", "v99", "v100", "v101", "v102", "v103", "v104", "v105", "v106", "v107", "v108", "v109", "v110", "v111", "v112", "v113", "v114", "v115", "v116", "v117", "v118", "v119", "v120", "v121", "v122", "v123", "v124", "v125", "v126", "v127", "v128", "v129", "v130", "v131", "v132", "v133", "v134", "v135", "v136", "v137", "v138", "v139", "v140", "v141", "v142", "v143", "v144", "v145", "v146", "v147", "v148", "v149", "v150", "v151", "v152", "v153", "v154", "v155", "v156", "v157", "v158", "v159", "v160", "v161", "v162", "v163", "v164", "v165", "v166", "v167", "v168", "v169", "v170", "v171", "v172", "v173", "v174", "v175", "v176", "v177", "v178", "v179", "v180", "v181", "v182", "v183", "v184", "v185", "v186", "v187", "v188", "v189", "v190", "v191", "v192", "v193", "v194", "v195", "v196", "v197", "v198", "v199", "v200", "v201", "v202", "v203", "v204", "v205", "v206", "v207", "v208", "v209", "v210", "v211", "v212", "v213", "v214", "v215", "v216", "v217", "v218", "v219", "v220", "v221", "v222", "v223", "v224", "v225", "v226", "v227", "v228", "v229", "v230", "v231", "v232", "v233", "v234", "v235", "v236", "v237", "v238", "v239", "a0", "a1", "a2", "a3", "a4", "a5", "a6", "a7", "a8", "a9", "a10", "a11", "a12", "a13", "a14", "a15", "a16", "a17", "a18", "a19", "a20", "a21", "a22", "a23", "a24", "a25", "a26", "a27", "a28", "a29", "a30", "a31", "a32", "a33", "a34", "a35", "a36", "a37", "a38", "a39", "a40", "a41", "a42", "a43", "a44", "a45", "a46", "a47", "a48", "a49", "a50", "a51", "a52", "a53", "a54", "a55", "a56", "a57", "a58", "a59", "a60", "a61", "a62", "a63", "a64", "a65", "a66", "a67", "a68", "a69", "a70", "a71", "a72", "a73", "a74", "a75", "a76", "a77", "a78", "a79", "a80", "a81", "a82", "a83", "a84", "a85", "a86", "a87", "a88", "a89", "a90", "a91", "a92", "a93", "a94", "a95", "a96", "a97", "a98", "a99", "a100", "a101", "a102", "a103", "a104", "a105", "a106", "a107", "a108", "a109", "a110", "a111", "a112", "a113", "a114", "a115", "a116", "a117", "a118", "a119", "a120", "a121", "a122", "a123", "a124", "a125", "a126", "a127", "a128", "a129", "a130", "a131", "a132", "a133", "a134", "a135", "a136", "a137", "a138", "a139", "a140", "a141", "a142", "a143", "a144", "a145", "a146", "a147", "a148", "a149", "a150", "a151", "a152", "a153", "a154", "a155", "a156", "a157", "a158", "a159", "a160", "a161", "a162", "a163", "a164", "a165", "a166", "a167", "a168", "a169", "a170", "a171", "a172", "a173", "a174", "a175", "a176", "a177", "a178", "a179", "a180", "a181", "a182", "a183", "a184", "a185", "a186", "a187", "a188", "a189", "a190", "a191", "a192", "a193", "a194", "a195", "a196", "a197", "a198", "a199", "a200", "a201", "a202", "a203", "a204", "a205", "a206", "a207", "a208", "a209", "a210", "a211", "a212", "a213", "a214", "a215", "a216", "a217", "a218", "a219", "a220", "a221", "a222", "a223", "a224", "a225", "a226", "a227", "a228", "a229", "a230", "a231", "a232", "a233", "a234", "a235", "a236", "a237", "a238", "a239", "a240", "a241", "a242", "a243", "a244", "a245", "a246", "a247", "a248", "a249", "a250", "a251", "a252", "a253", "a254", "a255", "s36", "s37", "s38", "s39", "s40", "s41", "s42", "s43", "s44", "s45", "s46", "s47", "s48", "s49", "s50", "s51", "s52", "s53", "s54", "s55", "s56", "s57", "s58", "s59", "scc", "vcc", "memory");
}
}

extern "C" void kernel_launch(void* const* d_in, const int* in_sizes, int n_in, void* d_out, int out_size, void* d_ws, size_t ws_size, hipStream_t stream) {
  (void)in_sizes; (void)n_in; (void)out_size; (void)d_ws; (void)ws_size;
  (void)hipFuncSetAttribute((const void*)mha_softmax_fwd, hipFuncAttributeMaxDynamicSharedMemorySize, kLdsBytes);
  mha_softmax_fwd<<<256, 256, kLdsBytes, stream>>>(d_in[0], d_in[1], d_in[2], d_out);
}
```
